```python
import torch

def _softmax(x, axis=-1):
    return torch.softmax(x, dim=axis)

def _gelu(x):
    return torch.nn.functional.gelu(x, approximate='tanh')

def _silu(x):
    return torch.nn.functional.silu(x)

def _sigmoid(x):
    return torch.sigmoid(x)

def _layernorm(x, axis=-1, eps=1e-05):
    m = x.mean(dim=axis, keepdim=True)
    v = x.var(dim=axis, keepdim=True, unbiased=False)
    return (x - m) / torch.sqrt(v + eps)

def _scatter_add(tgt, idx, src, axis=0):
    return tgt.clone().index_add_(axis, idx, src)

def _segment_sum(src, seg, n):
    out = torch.zeros((n,) + src.shape[1:], dtype=src.dtype)
    return out.index_add_(0, seg, src)

def _pad(x, pw):
    flat = tuple((v for pair in reversed(pw) for v in pair))
    return torch.nn.functional.pad(x, flat)
kernel_name = 'concat_heads__sliding_block__memory_aug_recurrent_mem_tr__prompt'

def setup_inputs():

    _w = lambda a: (lambda t: t.to(torch.bfloat16) if t.is_floating_point() else t)(torch.as_tensor(a))

    def _A_setup():
        B, S, D = (1, 2048, 640)
        return {'x': _w((lambda *s: torch.randn(*s, dtype=torch.bfloat16) * 0.2)(B, S, D)), 'wqkv': _w((lambda *s: torch.randn(*s, dtype=torch.bfloat16) * 0.2)(D, 3 * D)), 'wo': _w((lambda *s: torch.randn(*s, dtype=torch.bfloat16) * 0.05)(D, D))}
    _A_in = _A_setup()

    def _B_setup():
        B, S, Dm = (1, 2048, 640)
        H, Dh = (32, 128)
        M, seg = (8, 32)
        seg = min(seg, S)
        S = S // seg * seg
        return {'x': _w((lambda *s: torch.randn(*s, dtype=torch.bfloat16) * 0.05)(B, S, Dm)), 'mem0': _w((lambda *s: torch.randn(*s, dtype=torch.bfloat16) * 1.6)(B, M, Dm)), 'Wq': _w((lambda *s: torch.randn(*s, dtype=torch.bfloat16) * 0.8)(Dm, H, Dh) * Dm ** (-0.5)), 'Wk': _w((lambda *s: torch.randn(*s, dtype=torch.bfloat16) * 0.8)(Dm, H, Dh) * Dm ** (-0.5)), 'Wv': _w((lambda *s: torch.randn(*s, dtype=torch.bfloat16) * 1.6)(Dm, H, Dh) * Dm ** (-0.5)), 'Wo': _w((lambda *s: torch.randn(*s, dtype=torch.bfloat16) * 1.6)(H, Dh, Dm) * (H * Dh) ** (-0.5)), 'seg_len': seg}
    _B_in = _B_setup()
    return {'A_x': _A_in['x'], 'A_wqkv': _A_in['wqkv'], 'A_wo': _A_in['wo'], 'B_x': _B_in['x'], 'B_mem0': _B_in['mem0'], 'B_Wq': _B_in['Wq'], 'B_Wk': _B_in['Wk'], 'B_Wv': _B_in['Wv'], 'B_Wo': _B_in['Wo'], 'B_seg_len': _B_in['seg_len']}

def reference(A_x, A_wqkv, A_wo, B_x, B_mem0, B_Wq, B_Wk, B_Wv, B_Wo, B_seg_len):

    def _A_ref(x, wqkv, wo):
        B, S, D = x.shape
        H = 32
        Dh = D // H
        W = 256
        qkv = (lambda x, *d: x.permute(*d))((x @ wqkv).reshape(B, S, 3, H, Dh), 2, 0, 3, 1, 4)
        q, k, v = (qkv[0], qkv[1], qkv[2])
        scores = torch.einsum('bhqd,bhkd->bhqk', q, k) * Dh ** (-0.5)
        idx = torch.arange(S)
        mask = (idx[:, None] - idx[None, :] >= 0) & (idx[:, None] - idx[None, :] < W)
        scores = torch.where(mask[None, None], scores, -1000000000.0)
        p = _softmax(scores, axis=-1)
        o = torch.einsum('bhqk,bhkd->bhqd', p, v)
        return (lambda x, *d: x.permute(*d))(o, 0, 2, 1, 3).reshape(B, S, D) @ wo

    def _B_ref(x, mem0, Wq, Wk, Wv, Wo, seg_len):
        B, S, Dm = x.shape
        H, Dh = (Wq.shape[1], Wq.shape[2])
        M = mem0.shape[1]
        scale = Dh ** (-0.5)
        n_seg = S // seg_len
        L = M + seg_len
        idx = torch.arange(L)
        mask = (idx[:, None] < M) | (idx[:, None] >= idx[None, :])
        mem = mem0
        outs = []
        for s in range(n_seg):
            xs = x[:, s * seg_len:(s + 1) * seg_len, :].clone()
            h = torch.concatenate([mem, xs], axis=1)
            q = torch.einsum('btd,dhe->bhte', h, Wq)
            k = torch.einsum('btd,dhe->bhte', h, Wk)
            v = torch.einsum('btd,dhe->bhte', h, Wv)
            scores = torch.einsum('bhqe,bhke->bhqk', q, k) * scale
            scores = torch.where(mask[None, None], scores, -1000000000.0)
            p = _softmax(scores, axis=-1)
            o = torch.einsum('bhqk,bhke->bhqe', p, v)
            o = torch.einsum('bhte,hed->btd', o, Wo)
            mem = o[:, :M, :].clone()
            outs.append(o[:, M:, :])
        return torch.concatenate(outs, axis=1)
    a = _A_ref(A_x, A_wqkv, A_wo)
    b = _B_ref(B_x, B_mem0, B_Wq, B_Wk, B_Wv, B_Wo, B_seg_len)
    return torch.concatenate([a, b], axis=-1)
```

```cpp
#include <hip/hip_runtime.h>
#include <hip/hip_bf16.h>
#include <stdint.h>
typedef __bf16 bf16;
typedef __bf16 bf16x8 __attribute__((ext_vector_type(8)));
typedef float f32x4 __attribute__((ext_vector_type(4)));
constexpr int S = 2048, DM = 640;

__global__ void transpose_k(const bf16* __restrict__ in, bf16* __restrict__ out, int R, int C) {
    __shared__ bf16 t[32][33];
    int c = blockIdx.x * 32 + (threadIdx.x & 31), r0 = blockIdx.y * 32;
    for (int i = threadIdx.x >> 5; i < 32; i += 8) t[i][threadIdx.x & 31] = in[(size_t)(r0 + i) * C + c];
    __syncthreads();
    int r = r0 + (threadIdx.x & 31), c0 = blockIdx.x * 32;
    for (int i = threadIdx.x >> 5; i < 32; i += 8) out[(size_t)(c0 + i) * R + r] = t[threadIdx.x & 31][i];
}
__global__ void __launch_bounds__(256) gemm_nt(const bf16* __restrict__ A, int lda, const bf16* __restrict__ BT, bf16* __restrict__ C, int ldc, int K) {
    const int lane = threadIdx.x & 63, w = threadIdx.x >> 6;
    const int m0 = blockIdx.y * 64 + (w >> 1) * 32, n0 = blockIdx.x * 64 + (w & 1) * 32;
    f32x4 acc[2][2] = {};
    const bf16* a0 = A + (size_t)(m0 + (lane & 15)) * lda + (lane >> 4) * 8;
    const bf16* b0 = BT + (size_t)(n0 + (lane & 15)) * K + (lane >> 4) * 8;
    for (int k = 0; k < K; k += 32) {
        bf16x8 a[2], b[2];
        a[0] = *(const bf16x8*)(a0 + k); a[1] = *(const bf16x8*)(a0 + (size_t)16 * lda + k);
        b[0] = *(const bf16x8*)(b0 + k); b[1] = *(const bf16x8*)(b0 + (size_t)16 * K + k);
        for (int i = 0; i < 2; ++i) for (int j = 0; j < 2; ++j) acc[i][j] = __builtin_amdgcn_mfma_f32_16x16x32_bf16(a[i], b[j], acc[i][j], 0, 0, 0);
    }
    for (int i = 0; i < 2; ++i) for (int j = 0; j < 2; ++j) for (int r = 0; r < 4; ++r)
        C[(size_t)(m0 + 16 * i + (lane >> 4) * 4 + r) * ldc + n0 + 16 * j + (lane & 15)] = (bf16)acc[i][j][r];
}
__global__ void __launch_bounds__(256) attn_a(const bf16* __restrict__ qkv, bf16* __restrict__ o) {
    const int q = blockIdx.x * 256 + threadIdx.x, h = blockIdx.y;
    float qv[20], acc[20];
    for (int d = 0; d < 20; ++d) { qv[d] = (float)qkv[(size_t)q * 1920 + h * 20 + d] * 0.22360679774997896f; acc[d] = 0.f; }
    float m = -1e30f, l = 0.f;
    const int k0 = q - 255 > 0 ? q - 255 : 0;
    for (int k = k0; k <= q; ++k) {
        const bf16* kp = qkv + (size_t)k * 1920 + 640 + h * 20; const bf16* vp = kp + 640;
        float s = 0.f; for (int d = 0; d < 20; ++d) s += qv[d] * (float)kp[d];
        float mn = fmaxf(m, s), f = __expf(m - mn), p = __expf(s - mn);
        l = l * f + p; for (int d = 0; d < 20; ++d) acc[d] = acc[d] * f + p * (float)vp[d];
        m = mn;
    }
    for (int d = 0; d < 20; ++d) o[(size_t)q * 640 + h * 20 + d] = (bf16)(acc[d] / l);
}
__global__ void __launch_bounds__(256) rec_a(const bf16* __restrict__ mem, const bf16* __restrict__ WT, const bf16* __restrict__ WoT, const bf16* __restrict__ xqkv,
                                             bf16* __restrict__ kvm, float* __restrict__ part, int s) {
    __shared__ float qs[8][128], ks[8][128], vs[8][128], ps[8][40]; __shared__ bf16 ob[16][128];
    const int h = blockIdx.x, lane = threadIdx.x & 63, w = threadIdx.x >> 6;
    for (int t = w; t < 24; t += 4) {
        const int part_ = t >> 3, n0 = part_ * 4096 + h * 128 + (t & 7) * 16;
        f32x4 acc = {};
        for (int k = 0; k < 640; k += 32) {
            bf16x8 a = {}; if ((lane & 15) < 8) a = *(const bf16x8*)(mem + (lane & 15) * 640 + k + (lane >> 4) * 8);
            bf16x8 b = *(const bf16x8*)(WT + (size_t)(n0 + (lane & 15)) * 640 + k + (lane >> 4) * 8);
            acc = __builtin_amdgcn_mfma_f32_16x16x32_bf16(a, b, acc, 0, 0, 0);
        }
        if (lane < 32) for (int r = 0; r < 4; ++r) { float v = (float)(bf16)acc[r]; int i = (lane >> 4) * 4 + r, e = (t & 7) * 16 + (lane & 15); (part_ == 0 ? qs : part_ == 1 ? ks : vs)[i][e] = v; }
    }
    __syncthreads();
    for (int i = threadIdx.x; i < 8 * 128; i += 256) { kvm[((size_t)(s * 32 + h) * 2 + 0) * 1024 + i] = (bf16)ks[i >> 7][i & 127]; kvm[((size_t)(s * 32 + h) * 2 + 1) * 1024 + i] = (bf16)vs[i >> 7][i & 127]; }
    if (!part) return;
    for (int idx = threadIdx.x; idx < 320; idx += 256) { int i = idx / 40, j = idx % 40; float a = 0;
        if (j < 8) for (int e = 0; e < 128; ++e) a += qs[i][e] * ks[j][e];
        else { const bf16* kp = xqkv + (size_t)(s * 32 + j - 8) * 12288 + 4096 + h * 128; for (int e = 0; e < 128; ++e) a += qs[i][e] * (float)kp[e]; }
        ps[i][j] = a * 0.08838834764831845f; }
    __syncthreads();
    if (threadIdx.x < 8) { int i = threadIdx.x; float m = -1e30f; for (int j = 0; j < 40; ++j) m = fmaxf(m, ps[i][j]); float l = 0; for (int j = 0; j < 40; ++j) { float p = __expf(ps[i][j] - m); ps[i][j] = p; l += p; } for (int j = 0; j < 40; ++j) ps[i][j] = (float)(bf16)(ps[i][j] / l); }
    __syncthreads();
    for (int idx = threadIdx.x; idx < 16 * 128; idx += 256) { int i = idx >> 7, e = idx & 127; float a = 0;
        if (i < 8) { for (int j = 0; j < 8; ++j) a += ps[i][j] * vs[j][e]; for (int j = 0; j < 32; ++j) a += ps[i][8 + j] * (float)xqkv[(size_t)(s * 32 + j) * 12288 + 8192 + h * 128 + e]; }
        ob[i][e] = (bf16)a; }
    __syncthreads();
    for (int t = w; t < 40; t += 4) {
        f32x4 acc = {};
        for (int k = 0; k < 128; k += 32) {
            bf16x8 a = *(const bf16x8*)(&ob[lane & 15][k + (lane >> 4) * 8]);
            bf16x8 b = *(const bf16x8*)(WoT + (size_t)(t * 16 + (lane & 15)) * 4096 + h * 128 + k + (lane >> 4) * 8);
            acc = __builtin_amdgcn_mfma_f32_16x16x32_bf16(a, b, acc, 0, 0, 0);
        }
        if (lane < 32) for (int r = 0; r < 4; ++r) part[((size_t)h * 8 + (lane >> 4) * 4 + r) * 640 + t * 16 + (lane & 15)] = acc[r];
    }
}
__global__ void rec_b(const float* __restrict__ part, bf16* __restrict__ mem) {
    int i = blockIdx.x * 256 + threadIdx.x; if (i >= 5120) return;
    float a = 0; for (int h = 0; h < 32; ++h) a += part[(size_t)h * 5120 + i];
    mem[i] = (bf16)a;
}
__global__ void __launch_bounds__(256) attn_b(const bf16* __restrict__ xqkv, const bf16* __restrict__ kvm, bf16* __restrict__ o) {
    __shared__ float ps[32][41];
    const int s = blockIdx.x, h = blockIdx.y;
    const bf16* km = kvm + ((size_t)(s * 32 + h) * 2) * 1024; const bf16* vm = km + 1024;
    for (int idx = threadIdx.x; idx < 32 * 40; idx += 256) { int i = idx / 40, j = idx % 40; float a = -1e30f;
        const bf16* qp = xqkv + (size_t)(s * 32 + i) * 12288 + h * 128;
        if (j < 8 || j - 8 <= i) { const bf16* kp = j < 8 ? km + j * 128 : xqkv + (size_t)(s * 32 + j - 8) * 12288 + 4096 + h * 128; a = 0; for (int e = 0; e < 128; ++e) a += (float)qp[e] * (float)kp[e]; a *= 0.08838834764831845f; }
        ps[i][j] = a; }
    __syncthreads();
    if (threadIdx.x < 32) { int i = threadIdx.x; float m = -1e30f; for (int j = 0; j < 40; ++j) m = fmaxf(m, ps[i][j]); float l = 0; for (int j = 0; j < 40; ++j) { float p = __expf(ps[i][j] - m); ps[i][j] = p; l += p; } for (int j = 0; j < 40; ++j) ps[i][j] = (float)(bf16)(ps[i][j] / l); }
    __syncthreads();
    for (int idx = threadIdx.x; idx < 32 * 128; idx += 256) { int i = idx >> 7, e = idx & 127; float a = 0;
        for (int j = 0; j < 8; ++j) a += ps[i][j] * (float)vm[j * 128 + e];
        for (int j = 0; j <= i; ++j) a += ps[i][8 + j] * (float)xqkv[(size_t)(s * 32 + j) * 12288 + 8192 + h * 128 + e];
        o[(size_t)(s * 32 + i) * 4096 + h * 128 + e] = (bf16)a; }
}

extern "C" void kernel_launch(void* const* d_in, const int* in_sizes, int n_in, void* d_out, int out_size, void* d_ws, size_t ws_size, hipStream_t st) {
    const bf16 *Ax = (const bf16*)d_in[0], *Awqkv = (const bf16*)d_in[1], *Awo = (const bf16*)d_in[2];
    const bf16 *Bx = (const bf16*)d_in[3], *mem0 = (const bf16*)d_in[4], *Wq = (const bf16*)d_in[5], *Wk = (const bf16*)d_in[6], *Wv = (const bf16*)d_in[7], *Wo = (const bf16*)d_in[8];
    bf16* out = (bf16*)d_out;
    char* ws = (char*)d_ws; size_t off = 0;
    auto alloc = [&](size_t b) { char* p = ws + off; off += (b + 255) & ~(size_t)255; return p; };
    bf16* AwT = (bf16*)alloc(1920 * 640 * 2); bf16* AwoT = (bf16*)alloc(640 * 640 * 2);
    bf16* WT = (bf16*)alloc((size_t)12288 * 640 * 2); bf16* WoT = (bf16*)alloc((size_t)640 * 4096 * 2);
    bf16* qkvA = (bf16*)alloc((size_t)S * 1920 * 2); bf16* oA = (bf16*)alloc((size_t)S * 640 * 2);
    bf16* xqkv = (bf16*)alloc((size_t)S * 12288 * 2); bf16* oB = (bf16*)alloc((size_t)S * 4096 * 2);
    bf16* kvm = (bf16*)alloc((size_t)64 * 32 * 2 * 1024 * 2); float* part = (float*)alloc(32 * 5120 * 4); bf16* mem = (bf16*)alloc(5120 * 2);
    transpose_k<<<dim3(1920 / 32, 640 / 32), 256, 0, st>>>(Awqkv, AwT, 640, 1920);
    transpose_k<<<dim3(640 / 32, 640 / 32), 256, 0, st>>>(Awo, AwoT, 640, 640);
    transpose_k<<<dim3(4096 / 32, 640 / 32), 256, 0, st>>>(Wq, WT, 640, 4096);
    transpose_k<<<dim3(4096 / 32, 640 / 32), 256, 0, st>>>(Wk, WT + (size_t)4096 * 640, 640, 4096);
    transpose_k<<<dim3(4096 / 32, 640 / 32), 256, 0, st>>>(Wv, WT + (size_t)8192 * 640, 640, 4096);
    transpose_k<<<dim3(640 / 32, 4096 / 32), 256, 0, st>>>(Wo, WoT, 4096, 640);
    gemm_nt<<<dim3(1920 / 64, S / 64), 256, 0, st>>>(Ax, 640, AwT, qkvA, 1920, 640);
    attn_a<<<dim3(S / 256, 32), 256, 0, st>>>(qkvA, oA);
    gemm_nt<<<dim3(640 / 64, S / 64), 256, 0, st>>>(oA, 640, AwoT, out, 1280, 640);
    gemm_nt<<<dim3(12288 / 64, S / 64), 256, 0, st>>>(Bx, 640, WT, xqkv, 12288, 640);
    for (int s = 0; s < 64; ++s) {
        rec_a<<<32, 256, 0, st>>>(s == 0 ? mem0 : mem, WT, WoT, xqkv, kvm, s < 63 ? part : nullptr, s);
        if (s < 63) rec_b<<<20, 256, 0, st>>>(part, mem);
    }
    attn_b<<<dim3(64, 32), 256, 0, st>>>(xqkv, kvm, oB);
    gemm_nt<<<dim3(640 / 64, S / 64), 256, 0, st>>>(oB, 4096, WoT, out + 640, 1280, 4096);
}
```

```cpp
#include <hip/hip_runtime.h>
#include <stdint.h>

#define REC_ASM \
"s_mov_b64 s[40:41], %0\n" \
"s_mov_b32 s68, %1\n" \
"s_mov_b32 s69, %2\n" \
"s_mov_b32 s33, %3\n" \
"s_memtime s[30:31]\n" \
"s_memrealtime s[24:25]\n" \
"s_load_dwordx16 s[44:59], s[40:41], 0x0\n" \
"s_load_dwordx8 s[60:67], s[40:41], 0x40\n" \
"v_mbcnt_lo_u32_b32 v0, -1, 0\n" \
"v_mbcnt_hi_u32_b32 v0, -1, v0\n" \
"s_lshr_b32 s70, s68, 1\n" \
"s_and_b32 s71, s68, 1\n" \
"v_lshrrev_b32 v20, 4, v0\n" \
"v_and_b32 v21, 15, v0\n" \
"v_bfe_u32 v22, v0, 2, 2\n" \
"v_and_b32 v23, 3, v0\n" \
"v_and_b32 v24, 1, v20\n" \
"v_lshrrev_b32 v25, 1, v20\n" \
"s_waitcnt lgkmcnt(0)\n" \
"s_lshl_b32 s98, s69, 2\n" \
"v_lshlrev_b32 v26, 1, v24\n" \
"v_add_u32 v26, s98, v26\n" \
"v_xor_b32 v26, v26, v21\n" \
"v_lshlrev_b32 v26, 4, v26\n" \
"v_lshl_add_u32 v30, v20, 13, v26\n" \
"s_lshl_b32 s72, s70, 8\n" \
"s_lshl_b32 s73, s69, 15\n" \
"s_add_u32 s72, s72, s73\n" \
"v_lshrrev_b32 v26, 1, v22\n" \
"v_and_b32 v27, 1, v22\n" \
"v_lshlrev_b32 v28, 4, v25\n" \
"v_lshl_add_u32 v28, v24, 2, v28\n" \
"v_lshl_add_u32 v28, v26, 3, v28\n" \
"v_add_u32 v28, v28, v27\n" \
"v_lshl_add_u32 v29, v24, 1, v27\n" \
"v_lshl_add_u32 v29, v26, 2, v29\n" \
"v_lshlrev_b32 v29, 1, v29\n" \
"v_lshrrev_b32 v31, 1, v23\n" \
"s_lshl_b32 s98, s69, 2\n" \
"v_add_u32 v32, s98, v31\n" \
"v_add_u32 v33, 2, v32\n" \
"v_xor_b32 v32, v32, v29\n" \
"v_xor_b32 v33, v33, v29\n" \
"v_and_b32 v34, 1, v23\n" \
"v_lshlrev_b32 v34, 3, v34\n" \
"v_lshl_add_u32 v34, v28, 8, v34\n" \
"v_lshl_add_u32 v32, v32, 4, v34\n" \
"v_lshl_add_u32 v33, v33, 4, v34\n" \
"s_add_u32 s96, s44, s72\n" \
"s_addc_u32 s97, s45, 0\n" \
"s_lshl_b32 s98, s69, 10\n" \
"s_add_u32 s98, s98, 0\n" \
"s_mov_b32 s99, 20\n" \
"Lrec_sq0:\n" \
"s_mov_b32 m0, s98\n" \
"s_add_u32 s98, s98, 4096\n" \
"global_load_lds_dwordx4 v30, s[96:97]\n" \
"s_add_u32 s96, s96, 131072\n" \
"s_addc_u32 s97, s97, 0\n" \
"s_sub_u32 s99, s99, 1\n" \
"s_cmp_lg_u32 s99, 0\n" \
"s_cbranch_scc1 Lrec_sq0\n" \
"s_add_u32 s96, s44, s72\n" \
"s_addc_u32 s97, s45, 0\n" \
"s_add_u32 s96, s96, 2621440\n" \
"s_addc_u32 s97, s97, 0\n" \
"s_lshl_b32 s98, s69, 10\n" \
"s_add_u32 s98, s98, 81920\n" \
"s_mov_b32 s99, 20\n" \
"Lrec_sq1:\n" \
"s_mov_b32 m0, s98\n" \
"s_add_u32 s98, s98, 4096\n" \
"global_load_lds_dwordx4 v30, s[96:97]\n" \
"s_add_u32 s96, s96, 131072\n" \
"s_addc_u32 s97, s97, 0\n" \
"s_sub_u32 s99, s99, 1\n" \
"s_cmp_lg_u32 s99, 0\n" \
"s_cbranch_scc1 Lrec_sq1\n" \
"v_bfe_u32 v26, v0, 3, 1\n" \
"v_mul_u32_u24 v1, 0xaaaa, v26\n" \
"v_add_u32 v1, 0x4444, v1\n" \
"v_lshlrev_b32 v2, 4, v0\n" \
"s_mul_i32 s98, s69, 20480\n" \
"v_add_u32 v3, s98, v2\n" \
"v_add_u32 v4, 122880, v2\n" \
"v_lshrrev_b32 v37, 5, v0\n" \
"v_lshl_add_u32 v38, v37, 4, v21\n" \
"v_lshlrev_b32 v38, 4, v38\n" \
"v_lshl_add_u32 v38, v24, 3, v38\n" \
"s_lshl_b32 s98, s69, 9\n" \
"s_add_u32 s98, s98, 147456\n" \
"v_add_u32 v5, s98, v38\n" \
"v_lshl_add_u32 v38, v20, 2, v22\n" \
"v_lshlrev_b32 v38, 4, v38\n" \
"v_and_b32 v39, 1, v0\n" \
"v_lshl_add_u32 v38, v39, 3, v38\n" \
"v_add_u32 v6, 147456, v38\n" \
"v_lshlrev_b32 v38, 4, v21\n" \
"v_lshl_add_u32 v38, v24, 3, v38\n" \
"v_add_u32 v7, 151552, v38\n" \
"v_lshl_add_u32 v38, v37, 2, v38\n" \
"s_lshl_b32 s98, s69, 8\n" \
"s_add_u32 s98, s98, 151552\n" \
"v_add_u32 v16, s98, v38\n" \
"s_lshl_b32 s98, s69, 10\n" \
"s_add_u32 s98, s98, 152576\n" \
"v_add_u32 v17, s98, v2\n" \
"v_lshrrev_b32 v38, 2, v21\n" \
"v_lshl_add_u32 v38, v25, 2, v38\n" \
"s_lshl_b32 s98, s69, 3\n" \
"v_add_u32 v38, s98, v38\n" \
"v_lshlrev_b32 v38, 14, v38\n" \
"v_lshl_add_u32 v38, v24, 12, v38\n" \
"v_lshl_add_u32 v38, v23, 4, v38\n" \
"s_lshl_b32 s99, s68, 6\n" \
"v_add_u32 v8, s99, v38\n" \
"v_lshrrev_b32 v38, 3, v21\n" \
"v_lshl_add_u32 v38, v25, 1, v38\n" \
"s_lshl_b32 s98, s69, 2\n" \
"s_add_u32 s98, s98, 64\n" \
"v_add_u32 v38, s98, v38\n" \
"v_lshlrev_b32 v38, 14, v38\n" \
"v_lshl_add_u32 v38, v24, 12, v38\n" \
"v_lshl_add_u32 v38, v23, 4, v38\n" \
"v_bfe_u32 v39, v21, 2, 1\n" \
"v_lshl_add_u32 v38, v39, 3, v38\n" \
"v_add_u32 v9, s99, v38\n" \
"s_lshl_b32 s98, s69, 6\n" \
"s_add_u32 s98, s98, s68\n" \
"s_lshr_b32 s99, s68, 1\n" \
"s_add_u32 s99, s99, 128\n" \
"s_cmp_lt_u32 s69, 2\n" \
"s_cselect_b32 s98, s98, s99\n" \
"s_cselect_b32 s73, 1, 0\n" \
"s_cmp_eq_u32 s69, 2\n" \
"s_cselect_b32 s99, 1, 0\n" \
"s_xor_b32 s34, s71, 1\n" \
"s_and_b32 s99, s99, s34\n" \
"s_or_b32 s73, s73, s99\n" \
"s_min_u32 s98, s98, 159\n" \
"s_lshr_b32 s99, s98, 1\n" \
"s_and_b32 s34, s98, 1\n" \
"s_lshl_b32 s35, s99, 14\n" \
"s_lshl_b32 s96, s34, 12\n" \
"s_add_u32 s42, s35, s96\n" \
"s_lshl_b32 s35, s99, 7\n" \
"s_lshl_b32 s96, s34, 6\n" \
"s_add_u32 s35, s35, s96\n" \
"s_cmp_ge_u32 s35, 8192\n" \
"s_cselect_b32 s96, 8192, 0\n" \
"s_add_u32 s35, s35, s96\n" \
"v_and_b32 v38, 3, v0\n" \
"v_lshlrev_b32 v38, 4, v38\n" \
"v_bfe_u32 v39, v0, 2, 1\n" \
"v_lshl_add_u32 v39, v39, 1, v37\n" \
"v_lshl_add_u32 v38, v39, 2, v38\n" \
"v_add_u32 v11, s35, v38\n" \
"v_and_b32 v38, 7, v0\n" \
"v_and_b32 v39, 1, v38\n" \
"v_lshlrev_b32 v39, 4, v39\n" \
"v_mov_b32 v40, 0x3f80\n" \
"v_lshlrev_b32 v39, v39, v40\n" \
"v_lshrrev_b32 v38, 1, v38\n" \
"v_cmp_eq_u32 vcc, 0, v38\n" \
"s_nop 1\n" \
"v_cndmask_b32 v12, 0, v39, vcc\n" \
"v_cmp_eq_u32 vcc, 1, v38\n" \
"s_nop 1\n" \
"v_cndmask_b32 v13, 0, v39, vcc\n" \
"v_cmp_eq_u32 vcc, 2, v38\n" \
"s_nop 1\n" \
"v_cndmask_b32 v14, 0, v39, vcc\n" \
"v_cmp_eq_u32 vcc, 3, v38\n" \
"s_nop 1\n" \
"v_cndmask_b32 v15, 0, v39, vcc\n" \
"s_waitcnt vmcnt(20)\n" \
"s_barrier\n" \
"v_add_u32 v35, 0, v32\n" \
"v_add_u32 v36, 49152, v32\n" \
"ds_read_b64_tr_b16 a[0:1], v35 offset:0\n" \
"ds_read_b64_tr_b16 a[2:3], v35 offset:512\n" \
"ds_read_b64_tr_b16 a[4:5], v35 offset:8192\n" \
"ds_read_b64_tr_b16 a[6:7], v35 offset:8704\n" \
"ds_read_b64_tr_b16 a[8:9], v35 offset:16384\n" \
"ds_read_b64_tr_b16 a[10:11], v35 offset:16896\n" \
"ds_read_b64_tr_b16 a[12:13], v35 offset:24576\n" \
"ds_read_b64_tr_b16 a[14:15], v35 offset:25088\n" \
"ds_read_b64_tr_b16 a[16:17], v35 offset:32768\n" \
"ds_read_b64_tr_b16 a[18:19], v35 offset:33280\n" \
"ds_read_b64_tr_b16 a[20:21], v35 offset:40960\n" \
"ds_read_b64_tr_b16 a[22:23], v35 offset:41472\n" \
"ds_read_b64_tr_b16 a[24:25], v36 offset:0\n" \
"ds_read_b64_tr_b16 a[26:27], v36 offset:512\n" \
"ds_read_b64_tr_b16 a[28:29], v36 offset:8192\n" \
"ds_read_b64_tr_b16 a[30:31], v36 offset:8704\n" \
"ds_read_b64_tr_b16 a[32:33], v36 offset:16384\n" \
"ds_read_b64_tr_b16 a[34:35], v36 offset:16896\n" \
"ds_read_b64_tr_b16 a[36:37], v36 offset:24576\n" \
"ds_read_b64_tr_b16 a[38:39], v36 offset:25088\n" \
"v_add_u32 v35, 0, v33\n" \
"v_add_u32 v36, 49152, v33\n" \
"ds_read_b64_tr_b16 a[80:81], v35 offset:0\n" \
"ds_read_b64_tr_b16 a[82:83], v35 offset:512\n" \
"ds_read_b64_tr_b16 a[84:85], v35 offset:8192\n" \
"ds_read_b64_tr_b16 a[86:87], v35 offset:8704\n" \
"ds_read_b64_tr_b16 a[88:89], v35 offset:16384\n" \
"ds_read_b64_tr_b16 a[90:91], v35 offset:16896\n" \
"ds_read_b64_tr_b16 a[92:93], v35 offset:24576\n" \
"ds_read_b64_tr_b16 a[94:95], v35 offset:25088\n" \
"ds_read_b64_tr_b16 a[96:97], v35 offset:32768\n" \
"ds_read_b64_tr_b16 a[98:99], v35 offset:33280\n" \
"ds_read_b64_tr_b16 a[100:101], v35 offset:40960\n" \
"ds_read_b64_tr_b16 a[102:103], v35 offset:41472\n" \
"ds_read_b64_tr_b16 a[104:105], v36 offset:0\n" \
"ds_read_b64_tr_b16 a[106:107], v36 offset:512\n" \
"ds_read_b64_tr_b16 a[108:109], v36 offset:8192\n" \
"ds_read_b64_tr_b16 a[110:111], v36 offset:8704\n" \
"ds_read_b64_tr_b16 a[112:113], v36 offset:16384\n" \
"ds_read_b64_tr_b16 a[114:115], v36 offset:16896\n" \
"ds_read_b64_tr_b16 a[116:117], v36 offset:24576\n" \
"ds_read_b64_tr_b16 a[118:119], v36 offset:25088\n" \
"s_waitcnt lgkmcnt(0)\n" \
"s_barrier\n" \
"s_add_u32 s96, s46, s72\n" \
"s_addc_u32 s97, s47, 0\n" \
"s_lshl_b32 s98, s69, 10\n" \
"s_add_u32 s98, s98, 0\n" \
"s_mov_b32 s99, 20\n" \
"Lrec_sk0:\n" \
"s_mov_b32 m0, s98\n" \
"s_add_u32 s98, s98, 4096\n" \
"global_load_lds_dwordx4 v30, s[96:97]\n" \
"s_add_u32 s96, s96, 131072\n" \
"s_addc_u32 s97, s97, 0\n" \
"s_sub_u32 s99, s99, 1\n" \
"s_cmp_lg_u32 s99, 0\n" \
"s_cbranch_scc1 Lrec_sk0\n" \
"s_waitcnt vmcnt(20)\n" \
"s_barrier\n" \
"v_add_u32 v35, 81920, v32\n" \
"v_add_u32 v36, 131072, v32\n" \
"ds_read_b64_tr_b16 a[40:41], v35 offset:0\n" \
"ds_read_b64_tr_b16 a[42:43], v35 offset:512\n" \
"ds_read_b64_tr_b16 a[44:45], v35 offset:8192\n" \
"ds_read_b64_tr_b16 a[46:47], v35 offset:8704\n" \
"ds_read_b64_tr_b16 a[48:49], v35 offset:16384\n" \
"ds_read_b64_tr_b16 a[50:51], v35 offset:16896\n" \
"ds_read_b64_tr_b16 a[52:53], v35 offset:24576\n" \
"ds_read_b64_tr_b16 a[54:55], v35 offset:25088\n" \
"ds_read_b64_tr_b16 a[56:57], v35 offset:32768\n" \
"ds_read_b64_tr_b16 a[58:59], v35 offset:33280\n" \
"ds_read_b64_tr_b16 a[60:61], v35 offset:40960\n" \
"ds_read_b64_tr_b16 a[62:63], v35 offset:41472\n" \
"ds_read_b64_tr_b16 a[64:65], v36 offset:0\n" \
"ds_read_b64_tr_b16 a[66:67], v36 offset:512\n" \
"ds_read_b64_tr_b16 a[68:69], v36 offset:8192\n" \
"ds_read_b64_tr_b16 a[70:71], v36 offset:8704\n" \
"ds_read_b64_tr_b16 a[72:73], v36 offset:16384\n" \
"ds_read_b64_tr_b16 a[74:75], v36 offset:16896\n" \
"ds_read_b64_tr_b16 a[76:77], v36 offset:24576\n" \
"ds_read_b64_tr_b16 a[78:79], v36 offset:25088\n" \
"v_add_u32 v35, 81920, v33\n" \
"v_add_u32 v36, 131072, v33\n" \
"ds_read_b64_tr_b16 a[120:121], v35 offset:0\n" \
"ds_read_b64_tr_b16 a[122:123], v35 offset:512\n" \
"ds_read_b64_tr_b16 a[124:125], v35 offset:8192\n" \
"ds_read_b64_tr_b16 a[126:127], v35 offset:8704\n" \
"ds_read_b64_tr_b16 a[128:129], v35 offset:16384\n" \
"ds_read_b64_tr_b16 a[130:131], v35 offset:16896\n" \
"ds_read_b64_tr_b16 a[132:133], v35 offset:24576\n" \
"ds_read_b64_tr_b16 a[134:135], v35 offset:25088\n" \
"ds_read_b64_tr_b16 a[136:137], v35 offset:32768\n" \
"ds_read_b64_tr_b16 a[138:139], v35 offset:33280\n" \
"ds_read_b64_tr_b16 a[140:141], v35 offset:40960\n" \
"ds_read_b64_tr_b16 a[142:143], v35 offset:41472\n" \
"ds_read_b64_tr_b16 a[144:145], v36 offset:0\n" \
"ds_read_b64_tr_b16 a[146:147], v36 offset:512\n" \
"ds_read_b64_tr_b16 a[148:149], v36 offset:8192\n" \
"ds_read_b64_tr_b16 a[150:151], v36 offset:8704\n" \
"ds_read_b64_tr_b16 a[152:153], v36 offset:16384\n" \
"ds_read_b64_tr_b16 a[154:155], v36 offset:16896\n" \
"ds_read_b64_tr_b16 a[156:157], v36 offset:24576\n" \
"ds_read_b64_tr_b16 a[158:159], v36 offset:25088\n" \
"s_waitcnt lgkmcnt(0)\n" \
"s_barrier\n" \
"s_add_u32 s96, s46, s72\n" \
"s_addc_u32 s97, s47, 0\n" \
"s_add_u32 s96, s96, 2621440\n" \
"s_addc_u32 s97, s97, 0\n" \
"s_lshl_b32 s98, s69, 10\n" \
"s_add_u32 s98, s98, 81920\n" \
"s_mov_b32 s99, 20\n" \
"Lrec_sk1:\n" \
"s_mov_b32 m0, s98\n" \
"s_add_u32 s98, s98, 4096\n" \
"global_load_lds_dwordx4 v30, s[96:97]\n" \
"s_add_u32 s96, s96, 131072\n" \
"s_addc_u32 s97, s97, 0\n" \
"s_sub_u32 s99, s99, 1\n" \
"s_cmp_lg_u32 s99, 0\n" \
"s_cbranch_scc1 Lrec_sk1\n" \
"s_waitcnt vmcnt(20)\n" \
"s_barrier\n" \
"v_add_u32 v35, 0, v32\n" \
"v_add_u32 v36, 49152, v32\n" \
"ds_read_b64_tr_b16 a[160:161], v35 offset:0\n" \
"ds_read_b64_tr_b16 a[162:163], v35 offset:512\n" \
"ds_read_b64_tr_b16 a[164:165], v35 offset:8192\n" \
"ds_read_b64_tr_b16 a[166:167], v35 offset:8704\n" \
"ds_read_b64_tr_b16 a[168:169], v35 offset:16384\n" \
"ds_read_b64_tr_b16 a[170:171], v35 offset:16896\n" \
"ds_read_b64_tr_b16 a[172:173], v35 offset:24576\n" \
"ds_read_b64_tr_b16 a[174:175], v35 offset:25088\n" \
"ds_read_b64_tr_b16 a[176:177], v35 offset:32768\n" \
"ds_read_b64_tr_b16 a[178:179], v35 offset:33280\n" \
"ds_read_b64_tr_b16 a[180:181], v35 offset:40960\n" \
"ds_read_b64_tr_b16 a[182:183], v35 offset:41472\n" \
"ds_read_b64_tr_b16 a[184:185], v36 offset:0\n" \
"ds_read_b64_tr_b16 a[186:187], v36 offset:512\n" \
"ds_read_b64_tr_b16 a[188:189], v36 offset:8192\n" \
"ds_read_b64_tr_b16 a[190:191], v36 offset:8704\n" \
"ds_read_b64_tr_b16 a[192:193], v36 offset:16384\n" \
"ds_read_b64_tr_b16 a[194:195], v36 offset:16896\n" \
"ds_read_b64_tr_b16 a[196:197], v36 offset:24576\n" \
"ds_read_b64_tr_b16 a[198:199], v36 offset:25088\n" \
"v_add_u32 v35, 0, v33\n" \
"v_add_u32 v36, 49152, v33\n" \
"ds_read_b64_tr_b16 a[240:241], v35 offset:0\n" \
"ds_read_b64_tr_b16 a[242:243], v35 offset:512\n" \
"ds_read_b64_tr_b16 a[244:245], v35 offset:8192\n" \
"ds_read_b64_tr_b16 a[246:247], v35 offset:8704\n" \
"ds_read_b64_tr_b16 a[248:249], v35 offset:16384\n" \
"ds_read_b64_tr_b16 a[250:251], v35 offset:16896\n" \
"ds_read_b64_tr_b16 a[252:253], v35 offset:24576\n" \
"ds_read_b64_tr_b16 a[254:255], v35 offset:25088\n" \
"ds_read_b64_tr_b16 v[152:153], v35 offset:32768\n" \
"ds_read_b64_tr_b16 v[154:155], v35 offset:33280\n" \
"ds_read_b64_tr_b16 v[156:157], v35 offset:40960\n" \
"ds_read_b64_tr_b16 v[158:159], v35 offset:41472\n" \
"ds_read_b64_tr_b16 v[160:161], v36 offset:0\n" \
"ds_read_b64_tr_b16 v[162:163], v36 offset:512\n" \
"ds_read_b64_tr_b16 v[164:165], v36 offset:8192\n" \
"ds_read_b64_tr_b16 v[166:167], v36 offset:8704\n" \
"ds_read_b64_tr_b16 v[168:169], v36 offset:16384\n" \
"ds_read_b64_tr_b16 v[170:171], v36 offset:16896\n" \
"ds_read_b64_tr_b16 v[172:173], v36 offset:24576\n" \
"ds_read_b64_tr_b16 v[174:175], v36 offset:25088\n" \
"s_waitcnt lgkmcnt(0)\n" \
"s_barrier\n" \
"s_and_b32 s98, s69, 1\n" \
"s_lshl_b32 s98, s98, 1\n" \
"v_add_u32 v38, s98, v37\n" \
"v_lshlrev_b32 v38, 1, v38\n" \
"v_and_b32 v39, 7, v0\n" \
"v_xor_b32 v38, v38, v39\n" \
"v_lshlrev_b32 v38, 4, v38\n" \
"v_lshrrev_b32 v39, 3, v0\n" \
"v_lshl_add_u32 v30, v39, 13, v38\n" \
"s_lshl_b32 s72, s70, 8\n" \
"s_lshl_b32 s98, s71, 7\n" \
"s_add_u32 s72, s72, s98\n" \
"s_lshl_b32 s98, s69, 16\n" \
"s_add_u32 s72, s72, s98\n" \
"s_add_u32 s96, s48, s72\n" \
"s_addc_u32 s97, s49, 0\n" \
"s_lshl_b32 s98, s69, 10\n" \
"s_add_u32 s98, s98, 0\n" \
"s_mov_b32 s99, 20\n" \
"Lrec_sv:\n" \
"s_mov_b32 m0, s98\n" \
"s_add_u32 s98, s98, 4096\n" \
"global_load_lds_dwordx4 v30, s[96:97]\n" \
"s_add_u32 s96, s96, 262144\n" \
"s_addc_u32 s97, s97, 0\n" \
"s_sub_u32 s99, s99, 1\n" \
"s_cmp_lg_u32 s99, 0\n" \
"s_cbranch_scc1 Lrec_sv\n" \
"s_waitcnt vmcnt(20)\n" \
"s_barrier\n" \
"v_add_u32 v35, 81920, v32\n" \
"v_add_u32 v36, 131072, v32\n" \
"ds_read_b64_tr_b16 a[200:201], v35 offset:0\n" \
"ds_read_b64_tr_b16 a[202:203], v35 offset:512\n" \
"ds_read_b64_tr_b16 a[204:205], v35 offset:8192\n" \
"ds_read_b64_tr_b16 a[206:207], v35 offset:8704\n" \
"ds_read_b64_tr_b16 a[208:209], v35 offset:16384\n" \
"ds_read_b64_tr_b16 a[210:211], v35 offset:16896\n" \
"ds_read_b64_tr_b16 a[212:213], v35 offset:24576\n" \
"ds_read_b64_tr_b16 a[214:215], v35 offset:25088\n" \
"ds_read_b64_tr_b16 a[216:217], v35 offset:32768\n" \
"ds_read_b64_tr_b16 a[218:219], v35 offset:33280\n" \
"ds_read_b64_tr_b16 a[220:221], v35 offset:40960\n" \
"ds_read_b64_tr_b16 a[222:223], v35 offset:41472\n" \
"ds_read_b64_tr_b16 a[224:225], v36 offset:0\n" \
"ds_read_b64_tr_b16 a[226:227], v36 offset:512\n" \
"ds_read_b64_tr_b16 a[228:229], v36 offset:8192\n" \
"ds_read_b64_tr_b16 a[230:231], v36 offset:8704\n" \
"ds_read_b64_tr_b16 a[232:233], v36 offset:16384\n" \
"ds_read_b64_tr_b16 a[234:235], v36 offset:16896\n" \
"ds_read_b64_tr_b16 a[236:237], v36 offset:24576\n" \
"ds_read_b64_tr_b16 a[238:239], v36 offset:25088\n" \
"v_add_u32 v35, 81920, v33\n" \
"v_add_u32 v36, 131072, v33\n" \
"ds_read_b64_tr_b16 v[100:101], v35 offset:0\n" \
"ds_read_b64_tr_b16 v[102:103], v35 offset:512\n" \
"ds_read_b64_tr_b16 v[104:105], v35 offset:8192\n" \
"ds_read_b64_tr_b16 v[106:107], v35 offset:8704\n" \
"ds_read_b64_tr_b16 v[108:109], v35 offset:16384\n" \
"ds_read_b64_tr_b16 v[110:111], v35 offset:16896\n" \
"ds_read_b64_tr_b16 v[112:113], v35 offset:24576\n" \
"ds_read_b64_tr_b16 v[114:115], v35 offset:25088\n" \
"ds_read_b64_tr_b16 v[116:117], v35 offset:32768\n" \
"ds_read_b64_tr_b16 v[118:119], v35 offset:33280\n" \
"ds_read_b64_tr_b16 v[120:121], v35 offset:40960\n" \
"ds_read_b64_tr_b16 v[122:123], v35 offset:41472\n" \
"ds_read_b64_tr_b16 v[124:125], v36 offset:0\n" \
"ds_read_b64_tr_b16 v[126:127], v36 offset:512\n" \
"ds_read_b64_tr_b16 v[128:129], v36 offset:8192\n" \
"ds_read_b64_tr_b16 v[130:131], v36 offset:8704\n" \
"ds_read_b64_tr_b16 v[132:133], v36 offset:16384\n" \
"ds_read_b64_tr_b16 v[134:135], v36 offset:16896\n" \
"ds_read_b64_tr_b16 v[136:137], v36 offset:24576\n" \
"ds_read_b64_tr_b16 v[138:139], v36 offset:25088\n" \
"s_waitcnt lgkmcnt(0)\n" \
"s_barrier\n" \
"s_and_b32 s98, s69, 1\n" \
"s_lshl_b32 s98, s98, 3\n" \
"v_xor_b32 v30, s98, v0\n" \
"v_lshlrev_b32 v30, 4, v30\n" \
"s_lshl_b32 s72, s70, 7\n" \
"s_lshl_b32 s98, s71, 6\n" \
"s_add_u32 s72, s72, s98\n" \
"s_add_u32 s72, s72, s69\n" \
"s_mul_i32 s72, s72, 1280\n" \
"s_add_u32 s96, s50, s72\n" \
"s_addc_u32 s97, s51, 0\n" \
"s_lshl_b32 s98, s69, 10\n" \
"s_add_u32 s98, s98, 81920\n" \
"s_mov_b32 s99, 16\n" \
"Lrec_so:\n" \
"s_mov_b32 m0, s98\n" \
"s_add_u32 s98, s98, 4096\n" \
"global_load_lds_dwordx4 v30, s[96:97]\n" \
"s_add_u32 s96, s96, 5120\n" \
"s_addc_u32 s97, s97, 0\n" \
"s_sub_u32 s99, s99, 1\n" \
"s_cmp_lg_u32 s99, 0\n" \
"s_cbranch_scc1 Lrec_so\n" \
"s_lshl_b32 s72, s70, 7\n" \
"s_lshl_b32 s98, s71, 6\n" \
"s_add_u32 s72, s72, s98\n" \
"s_lshl_b32 s98, s69, 2\n" \
"s_add_u32 s72, s72, s98\n" \
"s_mul_i32 s72, s72, 1280\n" \
"v_lshlrev_b32 v38, 2, v24\n" \
"v_xor_b32 v38, v38, v21\n" \
"v_xor_b32 v39, 8, v38\n" \
"v_mul_u32_u24 v40, 1280, v20\n" \
"v_add_u32 v40, 1024, v40\n" \
"v_lshl_add_u32 v38, v38, 4, v40\n" \
"v_lshl_add_u32 v39, v39, 4, v40\n" \
"s_add_u32 s96, s50, s72\n" \
"s_addc_u32 s97, s51, 0\n" \
"s_lshl_b32 s98, s69, 10\n" \
"s_add_u32 s98, s98, 147456\n" \
"s_mov_b32 m0, s98\n" \
"s_add_u32 s98, s98, 4096\n" \
"global_load_lds_dwordx4 v38, s[96:97]\n" \
"s_add_u32 s96, s96, 20480\n" \
"s_addc_u32 s97, s97, 0\n" \
"s_mov_b32 m0, s98\n" \
"s_add_u32 s98, s98, 4096\n" \
"global_load_lds_dwordx4 v39, s[96:97]\n" \
"s_add_u32 s96, s96, 20480\n" \
"s_addc_u32 s97, s97, 0\n" \
"s_mov_b32 m0, s98\n" \
"s_add_u32 s98, s98, 4096\n" \
"global_load_lds_dwordx4 v38, s[96:97]\n" \
"s_add_u32 s96, s96, 20480\n" \
"s_addc_u32 s97, s97, 0\n" \
"s_mov_b32 m0, s98\n" \
"s_add_u32 s98, s98, 4096\n" \
"global_load_lds_dwordx4 v39, s[96:97]\n" \
"s_add_u32 s96, s96, 20480\n" \
"s_addc_u32 s97, s97, 0\n" \
"v_mov_b32 v43, 16384\n" \
"global_load_dword v42, v43, s[62:63] sc1\n" \
"v_lshrrev_b32 v26, 1, v22\n" \
"v_lshl_add_u32 v38, v26, 1, v24\n" \
"v_lshlrev_b32 v38, 1, v38\n" \
"s_lshl_b32 s98, s69, 1\n" \
"v_add_u32 v39, s98, v31\n" \
"v_xor_b32 v38, v38, v39\n" \
"v_and_b32 v39, 1, v23\n" \
"v_lshlrev_b32 v39, 3, v39\n" \
"v_lshl_add_u32 v39, v28, 7, v39\n" \
"v_lshl_add_u32 v35, v38, 4, v39\n" \
"v_add_u32 v35, 0, v35\n" \
"v_add_u32 v36, 40960, v35\n" \
"s_waitcnt vmcnt(21)\n" \
"s_barrier\n" \
"ds_read_b64_tr_b16 v[176:177], v35 offset:0\n" \
"ds_read_b64_tr_b16 v[178:179], v35 offset:256\n" \
"ds_read_b64_tr_b16 v[180:181], v35 offset:4096\n" \
"ds_read_b64_tr_b16 v[182:183], v35 offset:4352\n" \
"ds_read_b64_tr_b16 v[184:185], v35 offset:8192\n" \
"ds_read_b64_tr_b16 v[186:187], v35 offset:8448\n" \
"ds_read_b64_tr_b16 v[188:189], v35 offset:12288\n" \
"ds_read_b64_tr_b16 v[190:191], v35 offset:12544\n" \
"ds_read_b64_tr_b16 v[192:193], v35 offset:16384\n" \
"ds_read_b64_tr_b16 v[194:195], v35 offset:16640\n" \
"ds_read_b64_tr_b16 v[196:197], v35 offset:20480\n" \
"ds_read_b64_tr_b16 v[198:199], v35 offset:20736\n" \
"ds_read_b64_tr_b16 v[200:201], v35 offset:24576\n" \
"ds_read_b64_tr_b16 v[202:203], v35 offset:24832\n" \
"ds_read_b64_tr_b16 v[204:205], v35 offset:28672\n" \
"ds_read_b64_tr_b16 v[206:207], v35 offset:28928\n" \
"ds_read_b64_tr_b16 v[208:209], v35 offset:32768\n" \
"ds_read_b64_tr_b16 v[210:211], v35 offset:33024\n" \
"ds_read_b64_tr_b16 v[212:213], v35 offset:36864\n" \
"ds_read_b64_tr_b16 v[214:215], v35 offset:37120\n" \
"ds_read_b64_tr_b16 v[216:217], v36 offset:0\n" \
"ds_read_b64_tr_b16 v[218:219], v36 offset:256\n" \
"ds_read_b64_tr_b16 v[220:221], v36 offset:4096\n" \
"ds_read_b64_tr_b16 v[222:223], v36 offset:4352\n" \
"ds_read_b64_tr_b16 v[224:225], v36 offset:8192\n" \
"ds_read_b64_tr_b16 v[226:227], v36 offset:8448\n" \
"ds_read_b64_tr_b16 v[228:229], v36 offset:12288\n" \
"ds_read_b64_tr_b16 v[230:231], v36 offset:12544\n" \
"ds_read_b64_tr_b16 v[232:233], v36 offset:16384\n" \
"ds_read_b64_tr_b16 v[234:235], v36 offset:16640\n" \
"ds_read_b64_tr_b16 v[236:237], v36 offset:20480\n" \
"ds_read_b64_tr_b16 v[238:239], v36 offset:20736\n" \
"ds_read_b64_tr_b16 v[240:241], v36 offset:24576\n" \
"ds_read_b64_tr_b16 v[242:243], v36 offset:24832\n" \
"ds_read_b64_tr_b16 v[244:245], v36 offset:28672\n" \
"ds_read_b64_tr_b16 v[246:247], v36 offset:28928\n" \
"ds_read_b64_tr_b16 v[248:249], v36 offset:32768\n" \
"ds_read_b64_tr_b16 v[250:251], v36 offset:33024\n" \
"ds_read_b64_tr_b16 v[252:253], v36 offset:36864\n" \
"ds_read_b64_tr_b16 v[254:255], v36 offset:37120\n" \
"s_waitcnt lgkmcnt(0)\n" \
"s_barrier\n" \
"ds_write_b128 v3, v[176:179] offset:0\n" \
"ds_write_b128 v3, v[180:183] offset:1024\n" \
"ds_write_b128 v3, v[184:187] offset:2048\n" \
"ds_write_b128 v3, v[188:191] offset:3072\n" \
"ds_write_b128 v3, v[192:195] offset:4096\n" \
"ds_write_b128 v3, v[196:199] offset:5120\n" \
"ds_write_b128 v3, v[200:203] offset:6144\n" \
"ds_write_b128 v3, v[204:207] offset:7168\n" \
"ds_write_b128 v3, v[208:211] offset:8192\n" \
"ds_write_b128 v3, v[212:215] offset:9216\n" \
"ds_write_b128 v3, v[216:219] offset:10240\n" \
"ds_write_b128 v3, v[220:223] offset:11264\n" \
"ds_write_b128 v3, v[224:227] offset:12288\n" \
"ds_write_b128 v3, v[228:231] offset:13312\n" \
"ds_write_b128 v3, v[232:235] offset:14336\n" \
"ds_write_b128 v3, v[236:239] offset:15360\n" \
"ds_write_b128 v3, v[240:243] offset:16384\n" \
"ds_write_b128 v3, v[244:247] offset:17408\n" \
"ds_write_b128 v3, v[248:251] offset:18432\n" \
"ds_write_b128 v3, v[252:255] offset:19456\n" \
"v_lshrrev_b32 v26, 1, v22\n" \
"v_lshlrev_b32 v38, 4, v24\n" \
"v_lshl_add_u32 v38, v25, 2, v38\n" \
"v_add_u32 v38, v38, v27\n" \
"s_lshl_b32 s98, s69, 3\n" \
"v_lshl_add_u32 v39, v26, 2, v23\n" \
"v_add_u32 v39, s98, v39\n" \
"v_lshlrev_b32 v40, 3, v27\n" \
"v_xor_b32 v39, v39, v40\n" \
"v_lshlrev_b32 v39, 4, v39\n" \
"v_lshl_add_u32 v35, v38, 10, v39\n" \
"v_add_u32 v35, 81920, v35\n" \
"v_add_u32 v36, 32768, v35\n" \
"v_lshl_add_u32 v38, v24, 2, v25\n" \
"v_lshlrev_b32 v38, 10, v38\n" \
"v_lshl_add_u32 v38, v27, 8, v38\n" \
"v_lshl_add_u32 v39, v24, 1, v27\n" \
"v_lshlrev_b32 v39, 2, v39\n" \
"s_lshl_b32 s98, s69, 2\n" \
"v_lshl_add_u32 v40, v26, 1, v31\n" \
"v_add_u32 v40, s98, v40\n" \
"v_xor_b32 v39, v39, v40\n" \
"v_lshl_add_u32 v38, v39, 4, v38\n" \
"v_and_b32 v39, 1, v23\n" \
"v_lshl_add_u32 v38, v39, 3, v38\n" \
"v_add_u32 v41, 147456, v38\n" \
"s_waitcnt vmcnt(0)\n" \
"s_mov_b32 s34, 0\n" \
"v_mov_b32 v38, 16384\n" \
"v_mov_b32 v39, v42\n" \
"s_branch Lrec_go1\n" \
"Lrec_go:\n" \
"global_load_dword v39, v38, s[62:63] sc1\n" \
"s_add_u32 s34, s34, 1\n" \
"s_waitcnt vmcnt(0)\n" \
"Lrec_go1:\n" \
"s_nop 0\n" \
"v_readfirstlane_b32 s98, v39\n" \
"s_sub_u32 s98, s98, s33\n" \
"s_sub_u32 s98, s98, 0x100\n" \
"s_cmp_lt_u32 s98, 8\n" \
"s_cbranch_scc1 Lrec_go_ok\n" \
"s_cmp_lt_u32 s34, 262144\n" \
"s_cbranch_scc1 Lrec_go\n" \
"s_branch Lrec_fail\n" \
"Lrec_go_ok:\n" \
"s_and_b32 s99, s98, 1\n" \
"s_lshl_b32 s99, s99, 13\n" \
"s_mov_b32 s35, s99\n" \
"s_add_u32 s74, s52, s99\n" \
"s_addc_u32 s75, s53, 0\n" \
"s_bfe_u32 s99, s98, 0x10001\n" \
"s_lshl_b32 s99, s99, 13\n" \
"s_add_u32 s99, s99, 0x200000\n" \
"s_add_u32 s76, s52, s99\n" \
"s_addc_u32 s77, s53, 0\n" \
"s_bfe_u32 s99, s98, 0x10002\n" \
"s_lshl_b32 s99, s99, 13\n" \
"s_add_u32 s99, s99, 4194304\n" \
"s_add_u32 s82, s52, s99\n" \
"s_addc_u32 s83, s53, 0\n" \
"s_add_u32 s84, s82, 16384\n" \
"s_addc_u32 s85, s83, 0\n" \
"s_load_dwordx4 s[44:47], s[40:41], 0x60\n" \
"s_lshl_b32 s98, s70, 13\n" \
"s_lshl_b32 s34, s69, 11\n" \
"s_add_u32 s99, s98, s34\n" \
"s_add_u32 s86, s54, s99\n" \
"s_addc_u32 s87, s55, 0\n" \
"s_lshl_b32 s34, s69, 10\n" \
"s_add_u32 s98, s98, s34\n" \
"s_lshl_b32 s99, s71, 12\n" \
"s_add_u32 s98, s98, s99\n" \
"s_add_u32 s88, s56, s98\n" \
"s_addc_u32 s89, s57, 0\n" \
"s_lshl_b32 s98, s70, 11\n" \
"s_lshl_b32 s99, s71, 10\n" \
"s_add_u32 s99, s99, s98\n" \
"s_add_u32 s90, s58, s99\n" \
"s_addc_u32 s91, s59, 0\n" \
"s_add_u32 s92, s60, s99\n" \
"s_addc_u32 s93, s61, 0\n" \
"s_lshl_b32 s98, s70, 6\n" \
"s_add_u32 s43, s98, 32768\n" \
"s_add_u32 s43, s43, s35\n" \
"v_and_b32 v19, 0x70, v2\n" \
"v_lshrrev_b32 v19, 2, v19\n" \
"v_add_u32 v19, s43, v19\n" \
"s_add_u32 s58, s35, 35840\n" \
"v_lshrrev_b32 v38, 2, v2\n" \
"v_subrev_u32 v38, 32, v38\n" \
"v_add_u32 v39, s58, v38\n" \
"v_cmp_gt_u32 vcc, 16, v38\n" \
"s_or_b32 s34, s68, s69\n" \
"s_cmp_eq_u32 s34, 0\n" \
"s_cselect_b64 s[98:99], vcc, 0\n" \
"s_mov_b64 vcc, s[98:99]\n" \
"s_nop 1\n" \
"v_cndmask_b32 v19, v19, v39, vcc\n" \
"s_lshr_b32 s98, s68, 4\n" \
"s_lshl_b32 s98, s98, 2\n" \
"s_add_u32 s43, s58, s98\n" \
"s_and_b32 s98, s68, 15\n" \
"s_cmp_eq_u32 s98, 4\n" \
"s_cselect_b32 s43, s43, 0\n" \
"s_cmp_eq_u32 s69, 3\n" \
"s_cselect_b32 s43, s43, 0\n" \
"s_lshr_b32 s61, s66, 16\n" \
"s_and_b32 s66, s66, 0xffff\n" \
"s_cmp_eq_u32 s61, 0\n" \
"s_cselect_b32 s43, 0, s43\n" \
"s_lshl_b32 s61, s61, 4\n" \
"v_cvt_f32_u32 v38, s61\n" \
"s_memtime s[96:97]\n" \
"s_memrealtime s[98:99]\n" \
"v_readfirstlane_b32 s61, v38\n" \
"s_waitcnt lgkmcnt(0)\n" \
"s_sub_u32 s96, s96, s30\n" \
"s_sub_u32 s98, s98, s24\n" \
"v_cvt_f32_u32 v38, s96\n" \
"v_cvt_f32_u32 v39, s98\n" \
"v_rcp_f32 v39, v39\n" \
"s_nop 1\n" \
"v_mul_f32 v38, v38, v39\n" \
"v_mul_f32 v38, 0x41800000, v38\n" \
"v_add_f32 v38, 0.5, v38\n" \
"v_cvt_u32_f32 v38, v38\n" \
"v_min_u32 v38, 400, v38\n" \
"v_max_u32 v38, 330, v38\n" \
"s_nop 0\n" \
"v_readfirstlane_b32 s49, v38\n" \
"s_mov_b32 s100, 0\n" \
"s_cmp_eq_u32 s43, 0\n" \
"s_cbranch_scc1 Lrec_nopw\n" \
"v_cvt_f32_u32 v38, s96\n" \
"v_cvt_f32_u32 v39, s98\n" \
"v_rcp_f32 v38, v38\n" \
"v_mul_f32 v39, s61, v39\n" \
"s_nop 0\n" \
"v_mul_f32 v38, v38, v39\n" \
"v_cvt_u32_f32 v38, v38\n" \
"v_add_u32 v38, 16, v38\n" \
"v_min_u32 v38, 3840, v38\n" \
"v_max_u32 v38, 3136, v38\n" \
"v_mov_b32 v39, s43\n" \
"s_mov_b64 exec, 1\n" \
"global_store_dword v39, v38, s[62:63] sc1\n" \
"s_mov_b64 exec, -1\n" \
"Lrec_nopw:\n" \
"s_lshl_b32 s98, s70, 7\n" \
"s_lshl_b32 s99, s71, 4\n" \
"s_add_u32 s98, s98, s99\n" \
"s_lshl_b32 s99, s69, 2\n" \
"s_add_u32 s98, s98, s99\n" \
"s_add_u32 s39, s98, 36864\n" \
"s_xor_b32 s35, s35, 8192\n" \
"s_add_u32 s39, s39, s35\n" \
"s_add_u32 s56, s62, s39\n" \
"s_addc_u32 s57, s63, 0\n" \
"s_cmp_eq_u32 s69, 3\n" \
"s_cselect_b32 s54, 1, 0\n" \
"s_mov_b32 s55, 0\n" \
"s_mov_b32 s72, 0\n" \
"s_or_b32 s27, s68, s69\n" \
"s_mov_b32 s36, 0\n" \
"s_mov_b32 s37, 0\n" \
"s_waitcnt lgkmcnt(0)\n" \
"s_mov_b32 s41, 0\n" \
"s_and_b32 s44, s44, 0xffff\n" \
"s_mul_i32 s26, s69, s44\n" \
"s_lshr_b32 s95, s67, 16\n" \
"s_and_b32 s67, s67, 0xffff\n" \
"s_and_b32 s44, s45, 0xffff\n" \
"s_lshr_b32 s45, s45, 16\n" \
"s_sub_i32 s45, 0, s45\n" \
"s_lshr_b32 s48, s46, 16\n" \
"s_sub_i32 s48, 0, s48\n" \
"s_and_b32 s46, s46, 0xffff\n" \
"s_lshr_b32 s33, s47, 16\n" \
"s_and_b32 s47, s47, 0xffff\n" \
"s_mov_b32 s34, 0\n" \
"global_load_dword v18, v19, s[62:63] sc1\n" \
"s_waitcnt lgkmcnt(0)\n" \
"s_barrier\n" \
"ds_read_b64_tr_b16 v[176:177], v35 offset:0\n" \
"ds_read_b64_tr_b16 v[178:179], v35 offset:2048\n" \
"ds_read_b64_tr_b16 v[180:181], v35 offset:8192\n" \
"ds_read_b64_tr_b16 v[182:183], v35 offset:10240\n" \
"ds_read_b64_tr_b16 v[184:185], v36 offset:0\n" \
"ds_read_b64_tr_b16 v[186:187], v36 offset:2048\n" \
"ds_read_b64_tr_b16 v[188:189], v36 offset:8192\n" \
"ds_read_b64_tr_b16 v[190:191], v36 offset:10240\n" \
"ds_read_b64_tr_b16 v[192:193], v35 offset:8\n" \
"ds_read_b64_tr_b16 v[194:195], v35 offset:2056\n" \
"ds_read_b64_tr_b16 v[196:197], v35 offset:8200\n" \
"ds_read_b64_tr_b16 v[198:199], v35 offset:10248\n" \
"ds_read_b64_tr_b16 v[200:201], v36 offset:8\n" \
"ds_read_b64_tr_b16 v[202:203], v36 offset:2056\n" \
"ds_read_b64_tr_b16 v[204:205], v36 offset:8200\n" \
"ds_read_b64_tr_b16 v[206:207], v36 offset:10248\n" \
"ds_read_b64_tr_b16 v[208:209], v35 offset:512\n" \
"ds_read_b64_tr_b16 v[210:211], v35 offset:2560\n" \
"ds_read_b64_tr_b16 v[212:213], v35 offset:8704\n" \
"ds_read_b64_tr_b16 v[214:215], v35 offset:10752\n" \
"ds_read_b64_tr_b16 v[216:217], v36 offset:512\n" \
"ds_read_b64_tr_b16 v[218:219], v36 offset:2560\n" \
"ds_read_b64_tr_b16 v[220:221], v36 offset:8704\n" \
"ds_read_b64_tr_b16 v[222:223], v36 offset:10752\n" \
"ds_read_b64_tr_b16 v[224:225], v35 offset:520\n" \
"ds_read_b64_tr_b16 v[226:227], v35 offset:2568\n" \
"ds_read_b64_tr_b16 v[228:229], v35 offset:8712\n" \
"ds_read_b64_tr_b16 v[230:231], v35 offset:10760\n" \
"ds_read_b64_tr_b16 v[232:233], v36 offset:520\n" \
"ds_read_b64_tr_b16 v[234:235], v36 offset:2568\n" \
"ds_read_b64_tr_b16 v[236:237], v36 offset:8712\n" \
"ds_read_b64_tr_b16 v[238:239], v36 offset:10760\n" \
"ds_read_b64_tr_b16 v[240:241], v41 offset:0\n" \
"ds_read_b64_tr_b16 v[242:243], v41 offset:512\n" \
"ds_read_b64_tr_b16 v[244:245], v41 offset:2048\n" \
"ds_read_b64_tr_b16 v[246:247], v41 offset:2560\n" \
"ds_read_b64_tr_b16 v[248:249], v41 offset:8192\n" \
"ds_read_b64_tr_b16 v[250:251], v41 offset:8704\n" \
"ds_read_b64_tr_b16 v[252:253], v41 offset:10240\n" \
"ds_read_b64_tr_b16 v[254:255], v41 offset:10752\n" \
"s_waitcnt lgkmcnt(0)\n" \
"s_barrier\n" \
"s_mul_i32 s98, s69, 10240\n" \
"s_sub_u32 s98, 81920, s98\n" \
"v_add_u32 v10, s98, v3\n" \
"ds_write_b128 v10, v[100:103] offset:0\n" \
"ds_write_b128 v10, v[104:107] offset:1024\n" \
"ds_write_b128 v10, v[108:111] offset:2048\n" \
"ds_write_b128 v10, v[112:115] offset:3072\n" \
"ds_write_b128 v10, v[116:119] offset:4096\n" \
"ds_write_b128 v10, v[120:123] offset:5120\n" \
"ds_write_b128 v10, v[124:127] offset:6144\n" \
"ds_write_b128 v10, v[128:131] offset:7168\n" \
"ds_write_b128 v10, v[132:135] offset:8192\n" \
"ds_write_b128 v10, v[136:139] offset:9216\n" \
"s_mov_b32 s34, 0\n" \
"s_branch Lrec_f01\n" \
"Lrec_f0:\n" \
"global_load_dword v18, v19, s[62:63] sc1\n" \
"s_add_u32 s34, s34, 1\n" \
"Lrec_f01:\n" \
"s_waitcnt vmcnt(0)\n" \
"v_cmp_eq_u32 vcc, 0, v18\n" \
"s_cbranch_vccz Lrec_f0ok\n" \
"s_sleep 2\n" \
"s_cmp_lt_u32 s34, 262144\n" \
"s_cbranch_scc1 Lrec_f0\n" \
"s_branch Lrec_fail\n" \
"Lrec_f0ok:\n" \
"s_mov_b32 s40, 122880\n" \
"s_lshl_b32 s98, s69, 11\n" \
"s_add_u32 s0, s40, s98\n" \
"s_lshl_b32 s98, s69, 10\n" \
"s_add_u32 s1, s40, s98\n" \
"s_add_u32 s1, s1, 8192\n" \
"s_mov_b32 m0, s0\n" \
"s_nop 0\n" \
"global_load_lds_dwordx4 v2, s[86:87] sc1\n" \
"global_load_lds_dwordx4 v2, s[86:87] offset:1024 sc1\n" \
"s_mov_b32 m0, s1\n" \
"s_nop 0\n" \
"global_load_lds_dwordx4 v2, s[88:89] sc1\n" \
"s_add_u32 s86, s86, 0x40000\n" \
"s_addc_u32 s87, s87, 0\n" \
"s_add_u32 s88, s88, 0x40000\n" \
"s_addc_u32 s89, s89, 0\n" \
"s_xor_b32 s40, s40, 258048\n" \
"s_waitcnt vmcnt(0) lgkmcnt(0)\n" \
"Lrec_step:\n" \
"s_add_u32 s98, s72, 2\n" \
"s_min_u32 s98, s98, 64\n" \
"v_cmp_gt_u32 s[24:25], s98, v18\n" \
"s_barrier\n" \
"s_cmp_eq_u32 s72, 0\n" \
"s_cbranch_scc1 Lrec_first\n" \
"s_cmp_eq_u32 s73, 0\n" \
"s_cbranch_scc1 Lrec_notask\n" \
"s_add_u32 s80, s76, s42\n" \
"s_addc_u32 s81, s77, 0\n" \
"Lrec_u1_r:\n" \
"s_memrealtime s[96:97]\n" \
"s_waitcnt lgkmcnt(0)\n" \
"s_sub_i32 s98, s94, s96\n" \
"s_mul_i32 s98, s98, s49\n" \
"s_ashr_i32 s98, s98, 4\n" \
"s_add_i32 s98, s98, s26\n" \
"s_sub_i32 s98, s98, 60\n" \
"s_cmp_lt_i32 s98, s45\n" \
"s_cselect_b32 s98, s44, s98\n" \
"s_ashr_i32 s98, s98, 5\n" \
"s_cmp_gt_i32 s98, 24\n" \
"s_cbranch_scc0 Lrec_u1_s\n" \
"s_sleep 4\n" \
"s_branch Lrec_u1_r\n" \
"Lrec_u1_s:\n" \
"s_cmp_lt_i32 s98, 1\n" \
"s_cbranch_scc1 Lrec_u1_e\n" \
".p2align 4\n" \
"Lrec_u1_l:\n" \
"s_sub_u32 s98, s98, 1\n" \
"s_cmp_lg_u32 s98, 0\n" \
"s_cbranch_scc1 Lrec_u1_l\n" \
"Lrec_u1_e:\n" \
"global_load_dwordx4 v[20:23], v2, s[80:81] sc1\n" \
"global_load_dwordx4 v[24:27], v2, s[80:81] offset:1024 sc1\n" \
"global_load_dwordx4 v[28:31], v2, s[80:81] offset:2048 sc1\n" \
"global_load_dwordx4 v[32:35], v2, s[80:81] offset:3072 sc1\n" \
"s_add_u32 s37, s37, 1\n" \
"s_mov_b32 s34, 0\n" \
"ds_read_b128 v[60:63], v3 offset:0\n" \
"ds_read_b128 v[64:67], v3 offset:1024\n" \
"ds_read_b128 v[68:71], v3 offset:2048\n" \
"ds_read_b128 v[72:75], v3 offset:3072\n" \
"ds_read_b128 v[76:79], v3 offset:4096\n" \
"ds_read_b128 v[80:83], v3 offset:5120\n" \
"ds_read_b128 v[84:87], v3 offset:6144\n" \
"ds_read_b128 v[88:91], v3 offset:7168\n" \
"ds_read_b128 v[92:95], v3 offset:8192\n" \
"ds_read_b128 v[96:99], v3 offset:9216\n" \
"ds_read_b128 v[120:123], v4 offset:0\n" \
"ds_read_b128 v[124:127], v4 offset:1024\n" \
"ds_read_b128 v[128:131], v4 offset:2048\n" \
"ds_read_b128 v[132:135], v4 offset:3072\n" \
"ds_read_b128 v[136:139], v4 offset:4096\n" \
"ds_read_b128 v[140:143], v4 offset:5120\n" \
"ds_read_b128 v[144:147], v4 offset:6144\n" \
"ds_read_b128 v[148:151], v4 offset:7168\n" \
"Lrec_rwait:\n" \
"s_waitcnt vmcnt(3)\n" \
"v_mfma_f32_16x16x32_bf16 v[100:103], v[20:23], v[12:15], 0\n" \
"v_bitop3_b32 v105, v20, v21, s38 bitop3:0x7e\n" \
"v_bitop3_b32 v106, v22, v23, s38 bitop3:0x7e\n" \
"v_or_b32 v104, v105, v106\n" \
"s_waitcnt vmcnt(2)\n" \
"v_mfma_f32_16x16x32_bf16 v[100:103], v[24:27], v[12:15], v[100:103]\n" \
"v_bitop3_b32 v105, v24, v25, s38 bitop3:0x7e\n" \
"v_bitop3_b32 v106, v26, v27, s38 bitop3:0x7e\n" \
"v_or3_b32 v104, v104, v105, v106\n" \
"s_waitcnt vmcnt(1)\n" \
"v_mfma_f32_16x16x32_bf16 v[100:103], v[28:31], v[12:15], v[100:103]\n" \
"v_bitop3_b32 v105, v28, v29, s38 bitop3:0x7e\n" \
"v_bitop3_b32 v106, v30, v31, s38 bitop3:0x7e\n" \
"v_or3_b32 v104, v104, v105, v106\n" \
"s_waitcnt vmcnt(0)\n" \
"v_mfma_f32_16x16x32_bf16 v[100:103], v[32:35], v[12:15], v[100:103]\n" \
"v_bitop3_b32 v105, v32, v33, s38 bitop3:0x7e\n" \
"v_bitop3_b32 v106, v34, v35, s38 bitop3:0x7e\n" \
"v_or3_b32 v104, v104, v105, v106\n" \
"v_and_b32 v104, 1, v104\n" \
"v_cmp_ne_u32 vcc, 0, v104\n" \
"s_cbranch_vccnz Lrec_rretry\n" \
"s_memrealtime s[96:97]\n" \
"s_nop 0\n" \
"v_permlane32_swap_b32 v100, v102\n" \
"v_permlane32_swap_b32 v101, v103\n" \
"v_add_f32 v100, v100, v102\n" \
"v_add_f32 v101, v101, v103\n" \
"v_mov_b32 v102, v100\n" \
"v_mov_b32 v103, v101\n" \
"s_nop 0\n" \
"v_permlane16_swap_b32 v100, v102\n" \
"v_permlane16_swap_b32 v101, v103\n" \
"v_add_f32 v100, v100, v102\n" \
"v_add_f32 v101, v101, v103\n" \
"v_cvt_pk_bf16_f32 v100, v100, v101\n" \
"v_min_u32 v100, -2, v100\n" \
"s_mov_b32 exec_lo, 0xff\n" \
"s_mov_b32 exec_hi, 0xff\n" \
"global_store_dword v11, v100, s[82:83] sc1\n" \
"s_xor_b32 s50, s82, 0x8000\n" \
"s_mov_b32 s51, s83\n" \
"v_mov_b32 v101, -1\n" \
"global_store_dword v11, v101, s[50:51] sc1\n" \
"s_mov_b64 exec, -1\n" \
"s_cmp_lg_u32 s27, 0\n" \
"s_cbranch_scc1 Lrec_nomaster\n" \
"s_waitcnt lgkmcnt(0)\n" \
"s_add_u32 s98, s94, s66\n" \
"s_add_u32 s99, s96, s47\n" \
"s_cmp_le_u32 s72, 8\n" \
"s_cselect_b32 s58, 40, 0\n" \
"s_add_u32 s99, s99, s58\n" \
"s_sub_i32 s34, s96, s94\n" \
"s_cmp_gt_i32 s34, s33\n" \
"s_cselect_b32 s98, s99, s98\n" \
"s_cselect_b32 s58, 16, 0\n" \
"s_min_u32 s98, s98, -2\n" \
"s_mov_b64 exec, 1\n" \
"v_mov_b32 v106, s98\n" \
"global_store_dword v2, v106, s[84:85] offset:2048 sc1\n" \
"s_xor_b32 s50, s84, 0x8000\n" \
"s_mov_b32 s51, s85\n" \
"v_mov_b32 v104, -1\n" \
"global_store_dword v2, v104, s[50:51] offset:2048 sc1\n" \
"s_mov_b64 exec, -1\n" \
"v_readlane_b32 s98, v18, 8\n" \
"v_readlane_b32 s99, v18, 9\n" \
"v_readlane_b32 s50, v18, 10\n" \
"v_readlane_b32 s51, v18, 11\n" \
"s_cmp_ge_u32 s72, 9\n" \
"s_cselect_b32 s58, s58, 0\n" \
"s_add_u32 s41, s41, s58\n" \
"s_max_u32 s98, s98, s99\n" \
"s_max_u32 s50, s50, s51\n" \
"s_max_u32 s98, s98, s50\n" \
"s_add_u32 s98, s98, s41\n" \
"s_cmp_lt_u32 s72, 8\n" \
"s_cselect_b32 s99, 128, 0\n" \
"s_add_u32 s98, s98, s99\n" \
"s_add_u32 s98, s98, s100\n" \
"s_lshr_b32 s66, s98, 4\n" \
"s_and_b32 s100, s98, 15\n" \
"Lrec_nomaster:\n" \
"Lrec_join:\n" \
"v_mov_b64 v[100:101], 0\n" \
"v_mov_b64 v[102:103], 0\n" \
"v_mov_b64 v[104:105], 0\n" \
"v_mov_b64 v[106:107], 0\n" \
"v_mov_b64 v[108:109], 0\n" \
"v_mov_b64 v[110:111], 0\n" \
"v_mov_b64 v[112:113], 0\n" \
"v_mov_b64 v[114:115], 0\n" \
"v_mov_b64 v[116:117], 0\n" \
"v_mov_b64 v[118:119], 0\n" \
"s_add_u32 s80, s82, 4096\n" \
"s_addc_u32 s81, s83, 0\n" \
"s_add_u32 s2, s74, 0x80000\n" \
"s_addc_u32 s3, s75, 0\n" \
"s_bfe_u32 s32, s72, 0x10001\n" \
"s_xor_b32 s32, s32, 1\n" \
"s_mov_b32 s78, 0\n" \
"s_mov_b32 s79, 0\n" \
"s_lshl_b32 s98, s69, 11\n" \
"s_add_u32 s0, s40, s98\n" \
"s_lshl_b32 s98, s69, 10\n" \
"s_add_u32 s1, s40, s98\n" \
"s_add_u32 s1, s1, 8192\n" \
"s_cmp_lg_u64 s[24:25], 0\n" \
"s_cbranch_scc1 Lrec_flagwait\n" \
"Lrec_flag_ok:\n" \
"s_add_u32 s36, s36, 1\n" \
"s_waitcnt lgkmcnt(0)\n" \
"s_barrier\n" \
"Lrec_u2_r:\n" \
"s_memrealtime s[96:97]\n" \
"s_waitcnt lgkmcnt(0)\n" \
"s_sub_i32 s98, s94, s96\n" \
"s_mul_i32 s98, s98, s49\n" \
"s_ashr_i32 s98, s98, 4\n" \
"s_add_i32 s98, s98, s67\n" \
"s_sub_i32 s98, s98, 60\n" \
"s_cmp_lt_i32 s98, s48\n" \
"s_cselect_b32 s98, s46, s98\n" \
"s_max_i32 s98, s98, s95\n" \
"s_ashr_i32 s98, s98, 5\n" \
"s_cmp_gt_i32 s98, 24\n" \
"s_cbranch_scc0 Lrec_u2_s\n" \
"s_sleep 4\n" \
"s_branch Lrec_u2_r\n" \
"Lrec_u2_s:\n" \
"s_cmp_lt_i32 s98, 1\n" \
"s_cbranch_scc1 Lrec_u2_e\n" \
".p2align 4\n" \
"Lrec_u2_l:\n" \
"s_sub_u32 s98, s98, 1\n" \
"s_cmp_lg_u32 s98, 0\n" \
"s_cbranch_scc1 Lrec_u2_l\n" \
"Lrec_u2_e:\n" \
"Lrec_aload:\n" \
"global_load_dwordx4 v[20:23], v2, s[82:83] offset:0 sc1\n" \
"s_nop 12\n" \
"global_load_dwordx4 v[24:27], v2, s[82:83] offset:1024 sc1\n" \
"s_nop 12\n" \
"global_load_dwordx4 v[28:31], v2, s[82:83] offset:2048 sc1\n" \
"s_nop 12\n" \
"global_load_dwordx4 v[32:35], v2, s[82:83] offset:3072 sc1\n" \
"s_nop 12\n" \
"global_load_dwordx4 v[36:39], v2, s[80:81] offset:0 sc1\n" \
"s_nop 12\n" \
"global_load_dwordx4 v[40:43], v2, s[80:81] offset:1024 sc1\n" \
"s_nop 12\n" \
"global_load_dwordx4 v[44:47], v2, s[80:81] offset:2048 sc1\n" \
"s_nop 12\n" \
"global_load_dwordx4 v[48:51], v2, s[80:81] offset:3072 sc1\n" \
"s_mov_b64 exec, 1\n" \
"global_load_dword v0, v2, s[84:85] offset:2048 sc1\n" \
"s_mov_b64 exec, -1\n" \
"s_nop 8\n" \
"global_load_dwordx4 v[52:55], v2, s[84:85] offset:0 sc1\n" \
"s_waitcnt vmcnt(9)\n" \
"v_smfmac_f32_16x16x64_bf16 v[100:103], v[20:23], a[0:7], v1\n" \
"global_load_dwordx4 v[56:59], v2, s[84:85] offset:1024 sc1\n" \
"v_smfmac_f32_16x16x64_bf16 v[104:107], v[20:23], a[80:87], v1\n" \
"s_cmp_ge_u32 s72, 62\n" \
"s_cbranch_scc1 Lrec_ep63\n" \
"Lrec_ep63r:\n" \
"v_smfmac_f32_16x16x64_bf16 v[108:111], v[20:23], a[160:167], v1\n" \
"v_cmp_u_f32 vcc, v100, v101\n" \
"v_smfmac_f32_16x16x64_bf16 v[112:115], v[20:23], a[240:247], v1\n" \
"v_cmp_u_f32 s[98:99], v102, v103\n" \
"v_smfmac_f32_16x16x64_bf16 v[116:119], v[20:23], v[60:67], v1\n" \
"s_or_b64 vcc, vcc, s[98:99]\n" \
"s_cbranch_scc1 Lrec_qchk0\n" \
"Lrec_qok0:\n" \
"s_waitcnt vmcnt(9)\n" \
"v_smfmac_f32_16x16x64_bf16 v[100:103], v[24:27], a[8:15], v1\n" \
"s_mov_b32 m0, s0\n" \
"ds_read_b128 v[60:63], v10 offset:0\n" \
"v_smfmac_f32_16x16x64_bf16 v[104:107], v[24:27], a[88:95], v1\n" \
"global_load_lds_dwordx4 v2, s[86:87] sc1\n" \
"ds_read_b128 v[64:67], v10 offset:1024\n" \
"v_smfmac_f32_16x16x64_bf16 v[108:111], v[24:27], a[168:175], v1\n" \
"v_smfmac_f32_16x16x64_bf16 v[112:115], v[24:27], a[248:255], v1\n" \
"v_smfmac_f32_16x16x64_bf16 v[116:119], v[24:27], v[68:75], v1\n" \
"ds_read_b128 v[68:71], v3 offset:10240\n" \
"s_waitcnt vmcnt(9)\n" \
"v_smfmac_f32_16x16x64_bf16 v[100:103], v[28:31], a[16:23], v1\n" \
"ds_read_b128 v[72:75], v3 offset:11264\n" \
"v_smfmac_f32_16x16x64_bf16 v[104:107], v[28:31], a[96:103], v1\n" \
"global_load_lds_dwordx4 v2, s[86:87] offset:1024 sc1\n" \
"v_smfmac_f32_16x16x64_bf16 v[108:111], v[28:31], a[176:183], v1\n" \
"v_smfmac_f32_16x16x64_bf16 v[112:115], v[28:31], v[152:159], v1\n" \
"v_smfmac_f32_16x16x64_bf16 v[116:119], v[28:31], v[76:83], v1\n" \
"ds_read_b128 v[76:79], v10 offset:2048\n" \
"s_waitcnt vmcnt(9)\n" \
"v_smfmac_f32_16x16x64_bf16 v[100:103], v[32:35], a[24:31], v1\n" \
"s_mov_b32 m0, s1\n" \
"ds_read_b128 v[80:83], v10 offset:3072\n" \
"v_smfmac_f32_16x16x64_bf16 v[104:107], v[32:35], a[104:111], v1\n" \
"global_load_lds_dwordx4 v2, s[88:89] sc1\n" \
"v_smfmac_f32_16x16x64_bf16 v[108:111], v[32:35], a[184:191], v1\n" \
"v_cmp_u_f32 vcc, v100, v101\n" \
"v_smfmac_f32_16x16x64_bf16 v[112:115], v[32:35], v[160:167], v1\n" \
"v_cmp_u_f32 s[98:99], v102, v103\n" \
"v_smfmac_f32_16x16x64_bf16 v[116:119], v[32:35], v[84:91], v1\n" \
"s_or_b64 vcc, vcc, s[98:99]\n" \
"s_cbranch_scc1 Lrec_qchk3\n" \
"Lrec_qok3:\n" \
"s_waitcnt vmcnt(9)\n" \
"v_smfmac_f32_16x16x64_bf16 v[100:103], v[36:39], a[32:39], v1\n" \
"v_mov_b32 v17, s32\n" \
"ds_read_b128 v[84:87], v3 offset:12288\n" \
"v_smfmac_f32_16x16x64_bf16 v[104:107], v[36:39], a[112:119], v1\n" \
"global_load_dword v18, v19, s[62:63] sc1\n" \
"ds_read_b128 v[88:91], v3 offset:13312\n" \
"v_smfmac_f32_16x16x64_bf16 v[108:111], v[36:39], a[192:199], v1\n" \
"v_smfmac_f32_16x16x64_bf16 v[112:115], v[36:39], v[168:175], v1\n" \
"v_smfmac_f32_16x16x64_bf16 v[116:119], v[36:39], v[92:99], v1\n" \
"ds_read_b128 v[92:95], v10 offset:4096\n" \
"s_waitcnt vmcnt(9)\n" \
"v_smfmac_f32_16x16x64_bf16 v[100:103], v[40:43], a[40:47], v1\n" \
"ds_read_b128 v[96:99], v10 offset:5120\n" \
"v_smfmac_f32_16x16x64_bf16 v[104:107], v[40:43], a[120:127], v1\n" \
"global_store_dword v8, v17, s[74:75] sc1\n" \
"v_smfmac_f32_16x16x64_bf16 v[108:111], v[40:43], a[200:207], v1\n" \
"s_waitcnt lgkmcnt(8)\n" \
"v_smfmac_f32_16x16x64_bf16 v[112:115], v[40:43], v[60:67], v1\n" \
"ds_read_b128 v[60:63], v3 offset:14336\n" \
"s_waitcnt lgkmcnt(7)\n" \
"v_smfmac_f32_16x16x64_bf16 v[116:119], v[40:43], v[68:75], v1\n" \
"ds_read_b128 v[64:67], v3 offset:15360\n" \
"s_waitcnt vmcnt(9)\n" \
"v_smfmac_f32_16x16x64_bf16 v[100:103], v[44:47], a[48:55], v1\n" \
"ds_read_b128 v[68:71], v10 offset:6144\n" \
"v_smfmac_f32_16x16x64_bf16 v[104:107], v[44:47], a[128:135], v1\n" \
"global_store_dword v8, v17, s[2:3] sc1\n" \
"ds_read_b128 v[72:75], v10 offset:7168\n" \
"v_smfmac_f32_16x16x64_bf16 v[108:111], v[44:47], a[208:215], v1\n" \
"s_waitcnt lgkmcnt(8)\n" \
"v_smfmac_f32_16x16x64_bf16 v[112:115], v[44:47], v[76:83], v1\n" \
"ds_read_b128 v[76:79], v3 offset:16384\n" \
"s_waitcnt lgkmcnt(7)\n" \
"v_smfmac_f32_16x16x64_bf16 v[116:119], v[44:47], v[84:91], v1\n" \
"ds_read_b128 v[80:83], v3 offset:17408\n" \
"s_waitcnt vmcnt(9)\n" \
"v_smfmac_f32_16x16x64_bf16 v[100:103], v[48:51], a[56:63], v1\n" \
"ds_read_b128 v[84:87], v10 offset:8192\n" \
"v_smfmac_f32_16x16x64_bf16 v[104:107], v[48:51], a[136:143], v1\n" \
"global_store_dword v9, v17, s[74:75] sc1\n" \
"ds_read_b128 v[88:91], v10 offset:9216\n" \
"v_smfmac_f32_16x16x64_bf16 v[108:111], v[48:51], a[216:223], v1\n" \
"s_waitcnt lgkmcnt(8)\n" \
"v_smfmac_f32_16x16x64_bf16 v[112:115], v[48:51], v[92:99], v1\n" \
"ds_read_b128 v[92:95], v3 offset:18432\n" \
"s_waitcnt lgkmcnt(7)\n" \
"v_smfmac_f32_16x16x64_bf16 v[116:119], v[48:51], v[60:67], v1\n" \
"ds_read_b128 v[96:99], v3 offset:19456\n" \
"s_waitcnt vmcnt(8)\n" \
"v_smfmac_f32_16x16x64_bf16 v[100:103], v[52:55], a[64:71], v1\n" \
"v_readfirstlane_b32 s94, v0\n" \
"v_smfmac_f32_16x16x64_bf16 v[104:107], v[52:55], a[144:151], v1\n" \
"s_waitcnt vmcnt(7)\n" \
"v_smfmac_f32_16x16x64_bf16 v[100:103], v[56:59], a[72:79], v1\n" \
"s_cmp_eq_u32 s94, -1\n" \
"v_mov_b32 v17, s72\n" \
"v_smfmac_f32_16x16x64_bf16 v[104:107], v[56:59], a[152:159], v1\n" \
"s_cbranch_scc1 Lrec_qchk8\n" \
"Lrec_qok8:\n" \
"v_smfmac_f32_16x16x64_bf16 v[108:111], v[52:55], a[224:231], v1\n" \
"s_waitcnt lgkmcnt(6)\n" \
"v_cmp_u_f32 vcc, v100, v101\n" \
"v_smfmac_f32_16x16x64_bf16 v[112:115], v[52:55], v[68:75], v1\n" \
"v_cmp_u_f32 s[98:99], v102, v103\n" \
"s_mov_b64 exec, s[54:55]\n" \
"global_store_dword v2, v17, s[56:57] sc1\n" \
"s_mov_b64 exec, -1\n" \
"v_smfmac_f32_16x16x64_bf16 v[108:111], v[56:59], a[232:239], v1\n" \
"s_or_b64 vcc, vcc, s[98:99]\n" \
"s_cbranch_scc1 Lrec_qchk9\n" \
"Lrec_qok9:\n" \
"v_permlane32_swap_b32 v100, v104\n" \
"s_waitcnt lgkmcnt(2)\n" \
"v_smfmac_f32_16x16x64_bf16 v[112:115], v[56:59], v[84:91], v1\n" \
"v_permlane32_swap_b32 v101, v105\n" \
"v_smfmac_f32_16x16x64_bf16 v[116:119], v[52:55], v[76:83], v1\n" \
"v_permlane32_swap_b32 v102, v106\n" \
"s_waitcnt lgkmcnt(0)\n" \
"v_smfmac_f32_16x16x64_bf16 v[116:119], v[56:59], v[92:99], v1\n" \
"v_permlane32_swap_b32 v103, v107\n" \
"v_add_f32 v100, v100, v104\n" \
"v_add_f32 v101, v101, v105\n" \
"v_add_f32 v102, v102, v106\n" \
"v_add_f32 v103, v103, v107\n" \
"v_cvt_pk_bf16_f32 v100, v100, v101\n" \
"v_cvt_pk_bf16_f32 v101, v102, v103\n" \
"ds_write_b64 v5, v[100:101]\n" \
"v_permlane32_swap_b32 v108, v112\n" \
"v_permlane32_swap_b32 v109, v113\n" \
"v_permlane32_swap_b32 v110, v114\n" \
"v_permlane32_swap_b32 v111, v115\n" \
"v_add_f32 v108, v108, v112\n" \
"v_add_f32 v109, v109, v113\n" \
"v_add_f32 v110, v110, v114\n" \
"v_add_f32 v111, v111, v115\n" \
"v_cvt_pk_bf16_f32 v108, v108, v109\n" \
"v_cvt_pk_bf16_f32 v109, v110, v111\n" \
"ds_write_b64 v5, v[108:109] offset:2048\n" \
"v_permlane32_swap_b32 v116, v118\n" \
"v_permlane32_swap_b32 v117, v119\n" \
"v_add_f32 v116, v116, v118\n" \
"v_add_f32 v117, v117, v119\n" \
"v_cvt_pk_bf16_f32 v116, v116, v117\n" \
"ds_write_b32 v16, v116\n" \
"s_cmp_eq_u32 s72, 63\n" \
"s_waitcnt lgkmcnt(0)\n" \
"s_barrier\n" \
"s_cbranch_scc1 Lrec_final\n" \
"ds_read_b64_tr_b16 v[20:21], v6 offset:0\n" \
"ds_read_b64_tr_b16 v[22:23], v6 offset:256\n" \
"ds_read_b64_tr_b16 v[24:25], v6 offset:512\n" \
"ds_read_b64_tr_b16 v[26:27], v6 offset:768\n" \
"ds_read_b64_tr_b16 v[28:29], v6 offset:1024\n" \
"ds_read_b64_tr_b16 v[30:31], v6 offset:1280\n" \
"ds_read_b64_tr_b16 v[32:33], v6 offset:1536\n" \
"ds_read_b64_tr_b16 v[34:35], v6 offset:1792\n" \
"v_mov_b32 v92, 0\n" \
"v_mov_b32 v93, 0xff800000\n" \
"v_cmp_gt_u32 vcc, 0x200, v2\n" \
"v_mov_b32 v96, 0x3f803f80\n" \
"v_mov_b32 v97, 0x3f803f80\n" \
"v_cndmask_b32 v92, v93, v92, vcc\n" \
"s_bfe_u32 s38, s72, 0x10001\n" \
"s_sub_u32 s38, 0, s38\n" \
"s_and_b32 s35, s38, 1\n" \
"v_mov_b32 v93, v92\n" \
"v_mov_b32 v94, v92\n" \
"v_mov_b32 v95, v92\n" \
"v_mov_b32 v98, 0x3f803f80\n" \
"v_mov_b32 v99, 0x3f803f80\n" \
"s_waitcnt lgkmcnt(6)\n" \
"v_mfma_f32_16x16x32_bf16 v[52:55], v[120:123], v[20:23], 0\n" \
"ds_read_b64_tr_b16 v[36:37], v6 offset:2048\n" \
"v_mov_b64 v[100:101], 0\n" \
"v_mfma_f32_16x16x32_bf16 v[56:59], v[136:139], v[20:23], 0\n" \
"ds_read_b64_tr_b16 v[38:39], v6 offset:2304\n" \
"s_waitcnt lgkmcnt(6)\n" \
"v_mfma_f32_16x16x32_bf16 v[52:55], v[124:127], v[24:27], v[52:55]\n" \
"ds_read_b64_tr_b16 v[40:41], v6 offset:2560\n" \
"v_mov_b64 v[102:103], 0\n" \
"v_mfma_f32_16x16x32_bf16 v[56:59], v[140:143], v[24:27], v[56:59]\n" \
"ds_read_b64_tr_b16 v[42:43], v6 offset:2816\n" \
"s_waitcnt lgkmcnt(6)\n" \
"v_mfma_f32_16x16x32_bf16 v[52:55], v[128:131], v[28:31], v[52:55]\n" \
"ds_read_b64_tr_b16 v[44:45], v6 offset:3072\n" \
"v_mov_b64 v[104:105], 0\n" \
"v_mfma_f32_16x16x32_bf16 v[56:59], v[144:147], v[28:31], v[56:59]\n" \
"ds_read_b64_tr_b16 v[46:47], v6 offset:3328\n" \
"s_waitcnt lgkmcnt(6)\n" \
"v_mfma_f32_16x16x32_bf16 v[52:55], v[132:135], v[32:35], v[52:55]\n" \
"ds_read_b64_tr_b16 v[48:49], v6 offset:3584\n" \
"v_mov_b64 v[106:107], 0\n" \
"v_mfma_f32_16x16x32_bf16 v[56:59], v[148:151], v[32:35], v[56:59]\n" \
"ds_read_b64_tr_b16 v[50:51], v6 offset:3840\n" \
"s_waitcnt lgkmcnt(6)\n" \
"v_mfma_f32_16x16x32_bf16 v[92:95], v[36:39], v[20:23], v[92:95]\n" \
"ds_read_b64 v[76:77], v7 offset:0\n" \
"s_waitcnt lgkmcnt(5)\n" \
"v_mfma_f32_16x16x32_bf16 v[92:95], v[40:43], v[24:27], v[92:95]\n" \
"ds_read_b64 v[78:79], v7 offset:256\n" \
"s_waitcnt lgkmcnt(4)\n" \
"v_mfma_f32_16x16x32_bf16 v[92:95], v[44:47], v[28:31], v[92:95]\n" \
"ds_read_b64 v[80:81], v7 offset:512\n" \
"v_max3_f32 v86, v52, v53, v54\n" \
"s_waitcnt lgkmcnt(3)\n" \
"v_mfma_f32_16x16x32_bf16 v[92:95], v[48:51], v[32:35], v[92:95]\n" \
"ds_read_b64 v[82:83], v7 offset:768\n" \
"v_max3_f32 v86, v86, v55, v56\n" \
"v_max3_f32 v86, v86, v57, v58\n" \
"v_max_f32 v86, v86, v59\n" \
"ds_read_b128 v[60:63], v4 offset:8192\n" \
"ds_read_b128 v[64:67], v4 offset:9216\n" \
"ds_read_b128 v[68:71], v4 offset:10240\n" \
"ds_read_b128 v[72:75], v4 offset:11264\n" \
"v_max3_f32 v86, v86, v92, v93\n" \
"v_max3_f32 v86, v86, v94, v95\n" \
"v_mov_b32 v87, v86\n" \
"v_mov_b32 v116, 0\n" \
"v_mov_b32 v117, 0\n" \
"v_permlane32_swap_b32 v86, v87\n" \
"v_max_f32 v86, v86, v87\n" \
"v_mov_b32 v87, v86\n" \
"v_mov_b32 v118, 0\n" \
"v_mov_b32 v119, 0\n" \
"v_permlane16_swap_b32 v86, v87\n" \
"v_max_f32 v86, v86, v87\n" \
"s_mov_b32 s98, 0x3e0293ee\n" \
"v_mul_f32 v86, s98, v86\n" \
"v_fma_f32 v92, v92, s98, -v86\n" \
"v_fma_f32 v93, v93, s98, -v86\n" \
"v_fma_f32 v94, v94, s98, -v86\n" \
"v_fma_f32 v95, v95, s98, -v86\n" \
"v_exp_f32 v92, v92\n" \
"v_exp_f32 v93, v93\n" \
"v_exp_f32 v94, v94\n" \
"v_exp_f32 v95, v95\n" \
"v_fma_f32 v52, v52, s98, -v86\n" \
"v_fma_f32 v53, v53, s98, -v86\n" \
"v_fma_f32 v54, v54, s98, -v86\n" \
"v_fma_f32 v55, v55, s98, -v86\n" \
"v_cvt_pk_bf16_f32 v92, v92, v93\n" \
"v_cvt_pk_bf16_f32 v93, v94, v95\n" \
"v_fma_f32 v56, v56, s98, -v86\n" \
"v_fma_f32 v57, v57, s98, -v86\n" \
"v_fma_f32 v58, v58, s98, -v86\n" \
"v_fma_f32 v59, v59, s98, -v86\n" \
"s_waitcnt lgkmcnt(0)\n" \
"v_mfma_f32_16x16x16_bf16 v[148:151], v[96:97], v[92:93], 0\n" \
"v_exp_f32 v52, v52\n" \
"v_mfma_f32_16x16x16_bf16 v[132:135], v[76:77], v[92:93], 0\n" \
"v_exp_f32 v53, v53\n" \
"v_mfma_f32_16x16x16_bf16 v[136:139], v[78:79], v[92:93], 0\n" \
"v_exp_f32 v54, v54\n" \
"v_mfma_f32_16x16x16_bf16 v[140:143], v[80:81], v[92:93], 0\n" \
"v_exp_f32 v55, v55\n" \
"v_mfma_f32_16x16x16_bf16 v[144:147], v[82:83], v[92:93], 0\n" \
"v_exp_f32 v56, v56\n" \
"v_exp_f32 v57, v57\n" \
"v_exp_f32 v58, v58\n" \
"v_exp_f32 v59, v59\n" \
"v_cvt_pk_bf16_f32 v52, v52, v53\n" \
"v_cvt_pk_bf16_f32 v53, v54, v55\n" \
"v_cvt_pk_bf16_f32 v54, v56, v57\n" \
"v_cvt_pk_bf16_f32 v55, v58, v59\n" \
"s_nop 1\n" \
"v_mfma_f32_16x16x32_bf16 v[148:151], v[96:99], v[52:55], v[148:151]\n" \
"v_mov_b64 v[108:109], 0\n" \
"v_mfma_f32_16x16x32_bf16 v[132:135], v[60:63], v[52:55], v[132:135]\n" \
"v_mov_b64 v[110:111], 0\n" \
"v_mfma_f32_16x16x32_bf16 v[136:139], v[64:67], v[52:55], v[136:139]\n" \
"v_mov_b64 v[112:113], 0\n" \
"v_mfma_f32_16x16x32_bf16 v[140:143], v[68:71], v[52:55], v[140:143]\n" \
"v_mov_b64 v[114:115], 0\n" \
"v_mfma_f32_16x16x32_bf16 v[144:147], v[72:75], v[52:55], v[144:147]\n" \
"v_rcp_f32 v120, v148\n" \
"s_nop 0\n" \
"v_pk_mul_f32 v[132:133], v[132:133], v[120:121] op_sel_hi:[1,0]\n" \
"v_pk_mul_f32 v[134:135], v[134:135], v[120:121] op_sel_hi:[1,0]\n" \
"v_cvt_pk_bf16_f32 v92, v132, v133\n" \
"v_cvt_pk_bf16_f32 v93, v134, v135\n" \
"v_pk_mul_f32 v[136:137], v[136:137], v[120:121] op_sel_hi:[1,0]\n" \
"v_pk_mul_f32 v[138:139], v[138:139], v[120:121] op_sel_hi:[1,0]\n" \
"v_cvt_pk_bf16_f32 v94, v136, v137\n" \
"v_cvt_pk_bf16_f32 v95, v138, v139\n" \
"v_pk_mul_f32 v[140:141], v[140:141], v[120:121] op_sel_hi:[1,0]\n" \
"v_pk_mul_f32 v[142:143], v[142:143], v[120:121] op_sel_hi:[1,0]\n" \
"v_cvt_pk_bf16_f32 v96, v140, v141\n" \
"v_cvt_pk_bf16_f32 v97, v142, v143\n" \
"v_smfmac_f32_16x16x64_bf16 v[100:103], v[92:95], v[176:183], v1\n" \
"v_pk_mul_f32 v[144:145], v[144:145], v[120:121] op_sel_hi:[1,0]\n" \
"v_pk_mul_f32 v[146:147], v[146:147], v[120:121] op_sel_hi:[1,0]\n" \
"v_smfmac_f32_16x16x64_bf16 v[104:107], v[92:95], v[192:199], v1\n" \
"v_cvt_pk_bf16_f32 v98, v144, v145\n" \
"v_cvt_pk_bf16_f32 v99, v146, v147\n" \
"v_smfmac_f32_16x16x64_bf16 v[108:111], v[92:95], v[208:215], v1\n" \
"s_nop 0\n" \
"v_smfmac_f32_16x16x64_bf16 v[100:103], v[96:99], v[184:191], v1\n" \
"v_smfmac_f32_16x16x64_bf16 v[104:107], v[96:99], v[200:207], v1\n" \
"v_smfmac_f32_16x16x64_bf16 v[108:111], v[96:99], v[216:223], v1\n" \
"v_smfmac_f32_16x16x64_bf16 v[112:115], v[92:95], v[224:231], v1\n" \
"v_cvt_pk_bf16_f32 v132, v100, v101\n" \
"v_cvt_pk_bf16_f32 v133, v102, v103\n" \
"v_and_or_b32 v132, v132, -2, s35\n" \
"v_and_or_b32 v133, v133, -2, s35\n" \
"v_smfmac_f32_16x16x64_bf16 v[112:115], v[96:99], v[232:239], v1\n" \
"v_cvt_pk_bf16_f32 v134, v104, v105\n" \
"v_cvt_pk_bf16_f32 v135, v106, v107\n" \
"v_and_or_b32 v134, v134, -2, s35\n" \
"v_and_or_b32 v135, v135, -2, s35\n" \
"v_smfmac_f32_16x16x64_bf16 v[116:119], v[92:95], v[240:247], v1\n" \
"global_store_dwordx4 v8, v[132:135], s[74:75] sc1\n" \
"v_smfmac_f32_16x16x64_bf16 v[116:119], v[96:99], v[248:255], v1\n" \
"v_cvt_pk_bf16_f32 v136, v108, v109\n" \
"v_cvt_pk_bf16_f32 v137, v110, v111\n" \
"v_and_or_b32 v136, v136, -2, s35\n" \
"v_and_or_b32 v137, v137, -2, s35\n" \
"v_cvt_pk_bf16_f32 v138, v112, v113\n" \
"v_cvt_pk_bf16_f32 v139, v114, v115\n" \
"v_and_or_b32 v138, v138, -2, s35\n" \
"v_and_or_b32 v139, v139, -2, s35\n" \
"global_store_dwordx4 v8, v[136:139], s[2:3] sc1\n" \
"s_nop 0\n" \
"v_cvt_pk_bf16_f32 v140, v116, v117\n" \
"v_cvt_pk_bf16_f32 v141, v118, v119\n" \
"v_and_or_b32 v140, v140, -2, s35\n" \
"v_and_or_b32 v141, v141, -2, s35\n" \
"global_store_dwordx2 v9, v[140:141], s[74:75] sc1\n" \
"s_cmp_lg_u32 s72, 0\n" \
"s_cbranch_scc1 Lrec_hasref\n" \
"s_memrealtime s[96:97]\n" \
"s_waitcnt lgkmcnt(0)\n" \
"s_add_u32 s94, s96, 24\n" \
"Lrec_hasref:\n" \
"s_mov_b64 s[98:99], s[74:75]\n" \
"s_mov_b64 s[74:75], s[76:77]\n" \
"s_mov_b64 s[76:77], s[98:99]\n" \
"s_xor_b32 s82, s82, 0x8000\n" \
"s_xor_b32 s84, s84, 0x8000\n" \
"s_add_u32 s72, s72, 1\n" \
"s_cmp_lt_u32 s72, 63\n" \
"s_cselect_b32 s98, 0x40000, 0\n" \
"s_add_u32 s86, s86, s98\n" \
"s_addc_u32 s87, s87, 0\n" \
"s_add_u32 s88, s88, s98\n" \
"s_addc_u32 s89, s89, 0\n" \
"s_xor_b32 s40, s40, 258048\n" \
"v_xor_b32 v4, 258048, v4\n" \
"s_waitcnt vmcnt(3)\n" \
"s_branch Lrec_step\n" \
"Lrec_ep63:\n" \
"v_mov_b32 v17, s72\n" \
"s_mov_b64 exec, s[54:55]\n" \
"global_store_dword v2, v17, s[56:57] sc1\n" \
"s_mov_b64 exec, -1\n" \
"s_branch Lrec_ep63r\n" \
"Lrec_notask:\n" \
"s_cmp_lg_u32 s69, 3\n" \
"s_cbranch_scc1 Lrec_nopub\n" \
"s_lshl_b32 s98, s71, 10\n" \
"v_add_u32 v44, s98, v6\n" \
"v_lshrrev_b32 v45, 8, v2\n" \
"v_lshlrev_b32 v46, 8, v45\n" \
"v_and_b32 v47, 0x70, v2\n" \
"v_lshl_add_u32 v46, v47, 1, v46\n" \
"v_bfe_u32 v58, v2, 7, 1\n" \
"v_lshl_add_u32 v46, v58, 3, v46\n" \
"v_add_u32 v46, 151552, v46\n" \
"ds_read_b64_tr_b16 v[48:49], v44 offset:2048\n" \
"ds_read_b64_tr_b16 v[50:51], v44 offset:2304\n" \
"ds_read_b64_tr_b16 v[52:53], v44 offset:2560\n" \
"ds_read_b64_tr_b16 v[54:55], v44 offset:2816\n" \
"ds_read2_b64 v[56:59], v46 offset0:0 offset1:2\n" \
"v_lshlrev_b32 v45, 7, v45\n" \
"v_or_b32 v45, v45, v47\n" \
"s_waitcnt lgkmcnt(0)\n" \
"global_store_dwordx4 v2, v[56:59], s[92:93] sc1\n" \
"s_mov_b32 exec_lo, 0x00ff00ff\n" \
"s_mov_b32 exec_hi, 0x00ff00ff\n" \
"global_store_dwordx4 v45, v[48:51], s[90:91] sc1\n" \
"global_store_dwordx4 v45, v[52:55], s[90:91] offset:512 sc1\n" \
"s_mov_b64 exec, -1\n" \
"s_add_u32 s90, s90, 0x10000\n" \
"s_addc_u32 s91, s91, 0\n" \
"s_add_u32 s92, s92, 0x10000\n" \
"s_addc_u32 s93, s93, 0\n" \
"s_cmp_eq_u32 s43, 0\n" \
"s_cbranch_scc1 Lrec_noclk\n" \
"s_memtime s[96:97]\n" \
"s_memrealtime s[98:99]\n" \
"s_waitcnt lgkmcnt(0)\n" \
"s_cmp_eq_u32 s72, 3\n" \
"s_cselect_b32 s52, s96, s52\n" \
"s_cselect_b32 s53, s98, s53\n" \
"s_cmp_lt_u32 s72, 8\n" \
"s_cbranch_scc1 Lrec_noclk\n" \
"s_sub_u32 s96, s96, s52\n" \
"s_sub_u32 s98, s98, s53\n" \
"s_add_u32 s98, s98, 4\n" \
"v_cvt_f32_u32 v20, s96\n" \
"v_cvt_f32_u32 v21, s98\n" \
"v_rcp_f32 v20, v20\n" \
"v_mul_f32 v21, s61, v21\n" \
"s_nop 0\n" \
"v_mul_f32 v20, v20, v21\n" \
"v_cvt_u32_f32 v20, v20\n" \
"v_min_u32 v20, 3840, v20\n" \
"v_max_u32 v20, 3136, v20\n" \
"v_mov_b32 v21, s43\n" \
"s_mov_b64 exec, 1\n" \
"global_store_dword v21, v20, s[62:63] sc1\n" \
"s_mov_b64 exec, -1\n" \
"Lrec_noclk:\n" \
"Lrec_nopub:\n" \
"ds_read_b128 v[60:63], v3 offset:0\n" \
"ds_read_b128 v[64:67], v3 offset:1024\n" \
"ds_read_b128 v[68:71], v3 offset:2048\n" \
"ds_read_b128 v[72:75], v3 offset:3072\n" \
"ds_read_b128 v[76:79], v3 offset:4096\n" \
"ds_read_b128 v[80:83], v3 offset:5120\n" \
"ds_read_b128 v[84:87], v3 offset:6144\n" \
"ds_read_b128 v[88:91], v3 offset:7168\n" \
"ds_read_b128 v[92:95], v3 offset:8192\n" \
"ds_read_b128 v[96:99], v3 offset:9216\n" \
"ds_read_b128 v[120:123], v4 offset:0\n" \
"ds_read_b128 v[124:127], v4 offset:1024\n" \
"ds_read_b128 v[128:131], v4 offset:2048\n" \
"ds_read_b128 v[132:135], v4 offset:3072\n" \
"ds_read_b128 v[136:139], v4 offset:4096\n" \
"ds_read_b128 v[140:143], v4 offset:5120\n" \
"ds_read_b128 v[144:147], v4 offset:6144\n" \
"ds_read_b128 v[148:151], v4 offset:7168\n" \
"s_branch Lrec_join\n" \
"Lrec_first:\n" \
"ds_read_b128 v[60:63], v3 offset:0\n" \
"ds_read_b128 v[64:67], v3 offset:1024\n" \
"ds_read_b128 v[68:71], v3 offset:2048\n" \
"ds_read_b128 v[72:75], v3 offset:3072\n" \
"ds_read_b128 v[76:79], v3 offset:4096\n" \
"ds_read_b128 v[80:83], v3 offset:5120\n" \
"ds_read_b128 v[84:87], v3 offset:6144\n" \
"ds_read_b128 v[88:91], v3 offset:7168\n" \
"ds_read_b128 v[92:95], v3 offset:8192\n" \
"ds_read_b128 v[96:99], v3 offset:9216\n" \
"ds_read_b128 v[120:123], v4 offset:0\n" \
"ds_read_b128 v[124:127], v4 offset:1024\n" \
"ds_read_b128 v[128:131], v4 offset:2048\n" \
"ds_read_b128 v[132:135], v4 offset:3072\n" \
"ds_read_b128 v[136:139], v4 offset:4096\n" \
"ds_read_b128 v[140:143], v4 offset:5120\n" \
"ds_read_b128 v[144:147], v4 offset:6144\n" \
"ds_read_b128 v[148:151], v4 offset:7168\n" \
"v_mov_b64 v[100:101], 0\n" \
"v_mov_b64 v[102:103], 0\n" \
"v_mov_b64 v[104:105], 0\n" \
"v_mov_b64 v[106:107], 0\n" \
"v_mov_b64 v[108:109], 0\n" \
"v_mov_b64 v[110:111], 0\n" \
"v_mov_b64 v[112:113], 0\n" \
"v_mov_b64 v[114:115], 0\n" \
"v_mov_b64 v[116:117], 0\n" \
"v_mov_b64 v[118:119], 0\n" \
"s_add_u32 s80, s82, 4096\n" \
"s_addc_u32 s81, s83, 0\n" \
"s_add_u32 s2, s74, 0x80000\n" \
"s_addc_u32 s3, s75, 0\n" \
"s_bfe_u32 s32, s72, 0x10001\n" \
"s_xor_b32 s32, s32, 1\n" \
"s_mov_b32 s78, 0\n" \
"s_mov_b32 s79, 0\n" \
"s_lshl_b32 s98, s69, 11\n" \
"s_add_u32 s0, s40, s98\n" \
"s_lshl_b32 s98, s69, 10\n" \
"s_add_u32 s1, s40, s98\n" \
"s_add_u32 s1, s1, 8192\n" \
"s_cmp_lg_u64 s[24:25], 0\n" \
"s_cbranch_scc1 Lrec_flagwait0\n" \
"Lrec_flag_ok0:\n" \
"s_add_u32 s36, s36, 1\n" \
"s_waitcnt lgkmcnt(0)\n" \
"s_branch Lrec_aload\n" \
"Lrec_rretry:\n" \
"s_add_u32 s34, s34, 1\n" \
"s_cmp_lt_u32 s34, 262144\n" \
"s_cbranch_scc0 Lrec_fail\n" \
"global_load_dwordx4 v[20:23], v2, s[80:81] sc1\n" \
"global_load_dwordx4 v[24:27], v2, s[80:81] offset:1024 sc1\n" \
"global_load_dwordx4 v[28:31], v2, s[80:81] offset:2048 sc1\n" \
"global_load_dwordx4 v[32:35], v2, s[80:81] offset:3072 sc1\n" \
"s_add_u32 s37, s37, 1\n" \
"s_branch Lrec_rwait\n" \
"Lrec_flagwait:\n" \
"s_mov_b32 s34, 0\n" \
"s_add_u32 s98, s72, 2\n" \
"s_min_u32 s98, s98, 64\n" \
"Lrec_flagpoll:\n" \
"global_load_dword v18, v19, s[62:63] sc1\n" \
"s_add_u32 s34, s34, 1\n" \
"s_waitcnt vmcnt(0)\n" \
"v_cmp_gt_u32 vcc, s98, v18\n" \
"s_cbranch_vccz Lrec_flag_ok\n" \
"s_sleep 4\n" \
"s_cmp_lt_u32 s34, 262144\n" \
"s_cbranch_scc1 Lrec_flagpoll\n" \
"s_branch Lrec_fail\n" \
"Lrec_flagwait0:\n" \
"s_mov_b32 s34, 0\n" \
"s_add_u32 s98, s72, 2\n" \
"s_min_u32 s98, s98, 64\n" \
"Lrec_flagpoll0:\n" \
"global_load_dword v18, v19, s[62:63] sc1\n" \
"s_add_u32 s34, s34, 1\n" \
"s_waitcnt vmcnt(0)\n" \
"v_cmp_gt_u32 vcc, s98, v18\n" \
"s_cbranch_vccz Lrec_flag_ok0\n" \
"s_sleep 4\n" \
"s_cmp_lt_u32 s34, 262144\n" \
"s_cbranch_scc1 Lrec_flagpoll0\n" \
"s_branch Lrec_fail\n" \
"Lrec_qchk0:\n" \
"s_cmp_lg_u32 s78, 0\n" \
"s_cbranch_scc1 Lrec_qok0\n" \
"s_branch Lrec_qredo\n" \
"Lrec_qchk3:\n" \
"s_cmp_lg_u32 s78, 0\n" \
"s_cbranch_scc1 Lrec_qok3\n" \
"s_branch Lrec_qredo\n" \
"Lrec_qchk8:\n" \
"s_cmp_lg_u32 s78, 0\n" \
"s_cbranch_scc1 Lrec_qok8\n" \
"s_branch Lrec_qredo\n" \
"Lrec_qchk9:\n" \
"s_cmp_lg_u32 s78, 0\n" \
"s_cbranch_scc1 Lrec_qok9\n" \
"s_branch Lrec_qredo\n" \
"Lrec_qredo:\n" \
"s_nop 9\n" \
"s_add_u32 s36, s36, 1\n" \
"s_add_u32 s79, s79, 1\n" \
"s_cmp_lt_u32 s79, 262144\n" \
"s_cbranch_scc0 Lrec_fail\n" \
"s_cmp_lt_u32 s79, 2\n" \
"s_cbranch_scc1 Lrec_qredo2\n" \
"s_waitcnt vmcnt(0)\n" \
"v_max3_u32 v104, v20, v21, v22\n" \
"v_readfirstlane_b32 s98, v0\n" \
"v_max3_u32 v104, v104, v23, v24\n" \
"v_max3_u32 v104, v104, v25, v26\n" \
"v_max3_u32 v104, v104, v27, v28\n" \
"v_max3_u32 v104, v104, v29, v30\n" \
"v_max3_u32 v104, v104, v31, v32\n" \
"v_max3_u32 v104, v104, v33, v34\n" \
"v_max3_u32 v104, v104, v35, v36\n" \
"v_max3_u32 v104, v104, v37, v38\n" \
"v_max3_u32 v104, v104, v39, v40\n" \
"v_max3_u32 v104, v104, v41, v42\n" \
"v_max3_u32 v104, v104, v43, v44\n" \
"v_max3_u32 v104, v104, v45, v46\n" \
"v_max3_u32 v104, v104, v47, v48\n" \
"v_max3_u32 v104, v104, v49, v50\n" \
"v_max3_u32 v104, v104, v51, v52\n" \
"v_max3_u32 v104, v104, v53, v54\n" \
"v_max3_u32 v104, v104, v55, v56\n" \
"v_max3_u32 v104, v104, v57, v58\n" \
"v_max_u32 v104, v104, v59\n" \
"s_cmp_eq_u32 s98, -1\n" \
"s_cbranch_scc1 Lrec_qredo2\n" \
"v_cmp_eq_u32 vcc, -1, v104\n" \
"s_cbranch_vccnz Lrec_qredo2\n" \
"s_mov_b32 s78, 1\n" \
"Lrec_qredo2:\n" \
"v_mov_b64 v[100:101], 0\n" \
"v_mov_b64 v[102:103], 0\n" \
"v_mov_b64 v[104:105], 0\n" \
"v_mov_b64 v[106:107], 0\n" \
"v_mov_b64 v[108:109], 0\n" \
"v_mov_b64 v[110:111], 0\n" \
"v_mov_b64 v[112:113], 0\n" \
"v_mov_b64 v[114:115], 0\n" \
"v_mov_b64 v[116:117], 0\n" \
"v_mov_b64 v[118:119], 0\n" \
"ds_read_b128 v[60:63], v3 offset:0\n" \
"ds_read_b128 v[64:67], v3 offset:1024\n" \
"ds_read_b128 v[68:71], v3 offset:2048\n" \
"ds_read_b128 v[72:75], v3 offset:3072\n" \
"ds_read_b128 v[76:79], v3 offset:4096\n" \
"ds_read_b128 v[80:83], v3 offset:5120\n" \
"ds_read_b128 v[84:87], v3 offset:6144\n" \
"ds_read_b128 v[88:91], v3 offset:7168\n" \
"ds_read_b128 v[92:95], v3 offset:8192\n" \
"ds_read_b128 v[96:99], v3 offset:9216\n" \
"s_waitcnt lgkmcnt(0)\n" \
"s_branch Lrec_aload\n" \
"Lrec_final:\n" \
"s_cmp_lg_u32 s69, 3\n" \
"s_cbranch_scc1 Lrec_done\n" \
"s_lshl_b32 s98, s71, 10\n" \
"v_add_u32 v44, s98, v6\n" \
"v_lshrrev_b32 v45, 8, v2\n" \
"v_lshlrev_b32 v46, 8, v45\n" \
"v_and_b32 v47, 0x70, v2\n" \
"v_lshl_add_u32 v46, v47, 1, v46\n" \
"v_bfe_u32 v58, v2, 7, 1\n" \
"v_lshl_add_u32 v46, v58, 3, v46\n" \
"v_add_u32 v46, 151552, v46\n" \
"ds_read_b64_tr_b16 v[48:49], v44 offset:2048\n" \
"ds_read_b64_tr_b16 v[50:51], v44 offset:2304\n" \
"ds_read_b64_tr_b16 v[52:53], v44 offset:2560\n" \
"ds_read_b64_tr_b16 v[54:55], v44 offset:2816\n" \
"ds_read2_b64 v[56:59], v46 offset0:0 offset1:2\n" \
"v_lshlrev_b32 v45, 7, v45\n" \
"v_or_b32 v45, v45, v47\n" \
"s_waitcnt lgkmcnt(0)\n" \
"v_min_u32 v48, -2, v48\n" \
"v_min_u32 v49, -2, v49\n" \
"v_min_u32 v50, -2, v50\n" \
"v_min_u32 v51, -2, v51\n" \
"v_min_u32 v52, -2, v52\n" \
"v_min_u32 v53, -2, v53\n" \
"v_min_u32 v54, -2, v54\n" \
"v_min_u32 v55, -2, v55\n" \
"v_min_u32 v56, -2, v56\n" \
"v_min_u32 v57, -2, v57\n" \
"v_min_u32 v58, -2, v58\n" \
"v_min_u32 v59, -2, v59\n" \
"s_nop 0\n" \
"global_store_dwordx4 v2, v[56:59], s[92:93] sc1\n" \
"s_mov_b32 exec_lo, 0x00ff00ff\n" \
"s_mov_b32 exec_hi, 0x00ff00ff\n" \
"global_store_dwordx4 v45, v[48:51], s[90:91] sc1\n" \
"global_store_dwordx4 v45, v[52:55], s[90:91] offset:512 sc1\n" \
"s_mov_b64 exec, -1\n" \
"s_add_u32 s90, s90, 0x10000\n" \
"s_addc_u32 s91, s91, 0\n" \
"s_add_u32 s92, s92, 0x10000\n" \
"s_addc_u32 s93, s93, 0\n" \
"s_branch Lrec_done\n" \
"Lrec_fail:\n" \
"s_mov_b64 exec, 1\n" \
"v_mov_b32 v20, 16448\n" \
"v_mov_b32 v21, 1\n" \
"global_store_dword v20, v21, s[62:63] sc1\n" \
"Lrec_done:\n" \
"s_memtime s[28:29]\n" \
"s_memrealtime s[96:97]\n" \
"s_waitcnt lgkmcnt(0)\n" \
"s_sub_u32 s28, s28, s30\n" \
"s_mov_b64 exec, 1\n" \
"s_lshl_b32 s98, s68, 2\n" \
"s_add_u32 s98, s98, s69\n" \
"s_lshl_b32 s98, s98, 4\n" \
"s_add_u32 s98, s98, 49152\n" \
"v_mov_b32 v20, s98\n" \
"v_mov_b32 v24, s36\n" \
"v_mov_b32 v25, s37\n" \
"v_mov_b32 v26, s28\n" \
"v_mov_b32 v27, s96\n" \
"global_store_dwordx4 v20, v[24:27], s[62:63] sc1\n" \
"s_mov_b64 exec, -1\n" \
"s_waitcnt vmcnt(0)\n" \
"s_endpgm\n" \
""
#define REC_CLOB "v255", "a255", "s33", "s40", "s41", "s68", "s69", "s99", "vcc", "scc", "memory"
#define REC_CALL asm volatile(REC_ASM :: "s"(kp), "s"(role), "s"(w), "s"(tag) : REC_CLOB); __builtin_unreachable();
#define A2_ASM \
"s_mov_b64 s[40:41], %0\n" \
"s_mov_b64 s[42:43], %1\n" \
"s_mov_b64 s[44:45], %2\n" \
"s_mov_b64 s[46:47], %3\n" \
"s_mov_b32 s52, %4\n" \
"s_mov_b32 s51, 0x3ea52b5c\n" \
"v_mbcnt_lo_u32_b32 v0, -1, 0\n" \
"v_mbcnt_hi_u32_b32 v0, -1, v0\n" \
"v_and_b32 v1, 31, v0\n" \
"v_lshrrev_b32 v2, 5, v0\n" \
"v_lshlrev_b32 v5, 4, v0\n" \
"v_mul_u32_u24 v6, 48, v1\n" \
"v_lshl_add_u32 v6, v2, 4, v6\n" \
"v_mul_u32_u24 v7, 1280, v1\n" \
"v_lshl_add_u32 v7, v2, 3, v7\n" \
"s_lshl_b32 s53, s52, 14\n" \
"v_add_u32 v10, s53, v5\n" \
"v_mov_b32 v12, 0\n" \
"v_mov_b32 v13, 0\n" \
"v_mov_b32 v14, 0\n" \
"v_mov_b32 v15, 0\n" \
"ds_write_b128 v10, v[12:15] offset:0\n" \
"ds_write_b128 v10, v[12:15] offset:1024\n" \
"ds_write_b128 v10, v[12:15] offset:2048\n" \
"ds_write_b128 v10, v[12:15] offset:3072\n" \
"ds_write_b128 v10, v[12:15] offset:4096\n" \
"ds_write_b128 v10, v[12:15] offset:5120\n" \
"ds_write_b128 v10, v[12:15] offset:6144\n" \
"ds_write_b128 v10, v[12:15] offset:7168\n" \
"ds_write_b128 v10, v[12:15] offset:8192\n" \
"ds_write_b128 v10, v[12:15] offset:9216\n" \
"ds_write_b128 v10, v[12:15] offset:10240\n" \
"ds_write_b128 v10, v[12:15] offset:11264\n" \
"ds_write_b128 v10, v[12:15] offset:12288\n" \
"ds_write_b128 v10, v[12:15] offset:13312\n" \
"ds_write_b128 v10, v[12:15] offset:14336\n" \
"ds_write_b128 v10, v[12:15] offset:15360\n" \
"v_lshlrev_b32 v11, 2, v2\n" \
"v_sub_u32 v11, v1, v11\n" \
"v_mov_b32 v9, 0xff800000\n" \
"v_cmp_gt_i32 vcc, 0, v11\n" \
"s_nop 1\n" \
"v_cndmask_b32_e64 v184, v9, 0, vcc\n" \
"v_cndmask_b32_e64 v200, 0, v9, vcc\n" \
"v_cmp_gt_i32 vcc, 1, v11\n" \
"s_nop 1\n" \
"v_cndmask_b32_e64 v185, v9, 0, vcc\n" \
"v_cndmask_b32_e64 v201, 0, v9, vcc\n" \
"v_cmp_gt_i32 vcc, 2, v11\n" \
"s_nop 1\n" \
"v_cndmask_b32_e64 v186, v9, 0, vcc\n" \
"v_cndmask_b32_e64 v202, 0, v9, vcc\n" \
"v_cmp_gt_i32 vcc, 3, v11\n" \
"s_nop 1\n" \
"v_cndmask_b32_e64 v187, v9, 0, vcc\n" \
"v_cndmask_b32_e64 v203, 0, v9, vcc\n" \
"v_cmp_gt_i32 vcc, 8, v11\n" \
"s_nop 1\n" \
"v_cndmask_b32_e64 v188, v9, 0, vcc\n" \
"v_cndmask_b32_e64 v204, 0, v9, vcc\n" \
"v_cmp_gt_i32 vcc, 9, v11\n" \
"s_nop 1\n" \
"v_cndmask_b32_e64 v189, v9, 0, vcc\n" \
"v_cndmask_b32_e64 v205, 0, v9, vcc\n" \
"v_cmp_gt_i32 vcc, 10, v11\n" \
"s_nop 1\n" \
"v_cndmask_b32_e64 v190, v9, 0, vcc\n" \
"v_cndmask_b32_e64 v206, 0, v9, vcc\n" \
"v_cmp_gt_i32 vcc, 11, v11\n" \
"s_nop 1\n" \
"v_cndmask_b32_e64 v191, v9, 0, vcc\n" \
"v_cndmask_b32_e64 v207, 0, v9, vcc\n" \
"v_cmp_gt_i32 vcc, 16, v11\n" \
"s_nop 1\n" \
"v_cndmask_b32_e64 v192, v9, 0, vcc\n" \
"v_cndmask_b32_e64 v208, 0, v9, vcc\n" \
"v_cmp_gt_i32 vcc, 17, v11\n" \
"s_nop 1\n" \
"v_cndmask_b32_e64 v193, v9, 0, vcc\n" \
"v_cndmask_b32_e64 v209, 0, v9, vcc\n" \
"v_cmp_gt_i32 vcc, 18, v11\n" \
"s_nop 1\n" \
"v_cndmask_b32_e64 v194, v9, 0, vcc\n" \
"v_cndmask_b32_e64 v210, 0, v9, vcc\n" \
"v_cmp_gt_i32 vcc, 19, v11\n" \
"s_nop 1\n" \
"v_cndmask_b32_e64 v195, v9, 0, vcc\n" \
"v_cndmask_b32_e64 v211, 0, v9, vcc\n" \
"v_cmp_gt_i32 vcc, 24, v11\n" \
"s_nop 1\n" \
"v_cndmask_b32_e64 v196, v9, 0, vcc\n" \
"v_cndmask_b32_e64 v212, 0, v9, vcc\n" \
"v_cmp_gt_i32 vcc, 25, v11\n" \
"s_nop 1\n" \
"v_cndmask_b32_e64 v197, v9, 0, vcc\n" \
"v_cndmask_b32_e64 v213, 0, v9, vcc\n" \
"v_cmp_gt_i32 vcc, 26, v11\n" \
"s_nop 1\n" \
"v_cndmask_b32_e64 v198, v9, 0, vcc\n" \
"v_cndmask_b32_e64 v214, 0, v9, vcc\n" \
"v_cmp_gt_i32 vcc, 27, v11\n" \
"s_nop 1\n" \
"v_cndmask_b32_e64 v199, v9, 0, vcc\n" \
"v_cndmask_b32_e64 v215, 0, v9, vcc\n" \
"v_mov_b32 v224, 0\n" \
"v_mov_b32 v225, 0\n" \
"v_mov_b32 v226, 0\n" \
"v_mov_b32 v227, 0\n" \
"v_mov_b32 v228, 0\n" \
"v_mov_b32 v229, 0\n" \
"v_mov_b32 v230, 0\n" \
"v_mov_b32 v231, 0\n" \
"s_waitcnt lgkmcnt(0)\n" \
"s_barrier\n" \
"s_lshl_b32 s53, s52, 6\n" \
"v_add_u32 v10, s53, v0\n" \
"v_lshrrev_b32 v11, 5, v10\n" \
"v_lshlrev_b32 v11, 12, v11\n" \
"v_mad_u32_u24 v11, v1, 48, v11\n" \
"v_mov_b32 v12, 0xff80\n" \
"ds_write_b16 v11, v12 offset:32808\n" \
"s_mul_i32 s53, s52, 1536\n" \
"s_add_u32 s40, s40, s53\n" \
"s_addc_u32 s41, s41, 0\n" \
"s_add_u32 s42, s42, s53\n" \
"s_addc_u32 s43, s43, 0\n" \
"s_add_u32 s44, s44, s53\n" \
"s_addc_u32 s45, s45, 0\n" \
"s_mul_i32 s53, s52, 40960\n" \
"s_add_u32 s46, s46, s53\n" \
"s_addc_u32 s47, s47, 0\n" \
"s_lshl_b32 s49, s52, 12\n" \
"s_mov_b32 m0, s49\n" \
"s_add_u32 s50, s49, 2048\n" \
"global_load_lds_dwordx4 v5, s[40:41] sc1\n" \
"global_load_dwordx4 v[224:227], v6, s[44:45] sc1\n" \
"s_mov_b32 exec_hi, 0\n" \
"global_load_lds_dwordx4 v5, s[40:41] offset:1024 sc1\n" \
"global_load_dwordx4 v[228:231], v6, s[44:45] offset:32 sc1\n" \
"s_mov_b32 exec_hi, -1\n" \
"s_add_u32 s40, s40, 6144\n" \
"s_addc_u32 s41, s41, 0\n" \
"s_add_u32 s44, s44, 6144\n" \
"s_addc_u32 s45, s45, 0\n" \
"s_add_u32 s49, s49, 16384\n" \
"s_add_u32 s53, s52, 8\n" \
"s_lshl_b32 s53, s53, 12\n" \
"v_add_u32 v3, s53, v6\n" \
"s_add_u32 s53, s52, 4\n" \
"s_lshl_b32 s53, s53, 12\n" \
"s_add_u32 s53, s53, 2048\n" \
"v_lshrrev_b32 v10, 3, v1\n" \
"v_xor_b32 v10, v10, v2\n" \
"v_and_b32 v10, 1, v10\n" \
"v_lshlrev_b32 v10, 4, v10\n" \
"v_lshl_add_u32 v10, v1, 5, v10\n" \
"v_add_u32 v4, s53, v10\n" \
"s_mov_b32 s48, -1\n" \
"s_waitcnt vmcnt(0) lgkmcnt(0)\n" \
"s_barrier\n" \
"ds_read_b128 a[8:11], v4\n" \
"ds_read_b128 a[12:15], v4 offset:768\n" \
"ds_read_b128 a[0:3], v3\n" \
"ds_read_b128 a[4:7], v3 offset:32\n" \
"La_loop%=:\n" \
"s_waitcnt vmcnt(3)\n" \
"s_barrier\n" \
"v_mov_b32 v216, v224\n" \
"v_mov_b32 v217, v225\n" \
"v_mov_b32 v218, v226\n" \
"v_mov_b32 v219, v227\n" \
"v_mov_b32 v220, v228\n" \
"v_mov_b32 v221, v229\n" \
"v_mov_b32 v222, v230\n" \
"v_mov_b32 v223, v231\n" \
"v_mov_b32 v10, 0xff800000\n" \
"v_mov_b32 v11, 0xff800000\n" \
"v_fma_f32 v16, v16, s51, -v8\n" \
"v_fma_f32 v17, v17, s51, -v8\n" \
"v_fma_f32 v18, v18, s51, -v8\n" \
"v_fma_f32 v19, v19, s51, -v8\n" \
"v_fma_f32 v20, v20, s51, -v8\n" \
"v_fma_f32 v21, v21, s51, -v8\n" \
"v_fma_f32 v22, v22, s51, -v8\n" \
"v_fma_f32 v23, v23, s51, -v8\n" \
"v_exp_f32 v16, v16\n" \
"v_exp_f32 v17, v17\n" \
"v_exp_f32 v18, v18\n" \
"v_exp_f32 v19, v19\n" \
"v_exp_f32 v20, v20\n" \
"v_exp_f32 v21, v21\n" \
"v_exp_f32 v22, v22\n" \
"v_exp_f32 v23, v23\n" \
"v_fma_f32 v24, v24, s51, -v8\n" \
"v_cvt_pk_bf16_f32 v160, v16, v17\n" \
"v_cvt_pk_bf16_f32 v161, v18, v19\n" \
"v_cvt_pk_bf16_f32 v162, v20, v21\n" \
"v_cvt_pk_bf16_f32 v163, v22, v23\n" \
"v_fma_f32 v25, v25, s51, -v8\n" \
"v_fma_f32 v26, v26, s51, -v8\n" \
"s_waitcnt lgkmcnt(2)\n" \
"v_mfma_f32_32x32x16_bf16 v[168:183], a[8:11], v[160:163], 0\n" \
"v_fma_f32 v27, v27, s51, -v8\n" \
"v_fma_f32 v28, v28, s51, -v8\n" \
"v_fma_f32 v29, v29, s51, -v8\n" \
"v_fma_f32 v30, v30, s51, -v8\n" \
"v_fma_f32 v31, v31, s51, -v8\n" \
"s_mov_b32 m0, s49\n" \
"s_nop 0\n" \
"global_load_lds_dwordx4 v5, s[40:41] sc1\n" \
"v_exp_f32 v24, v24\n" \
"v_exp_f32 v25, v25\n" \
"v_exp_f32 v26, v26\n" \
"v_exp_f32 v27, v27\n" \
"v_exp_f32 v28, v28\n" \
"v_exp_f32 v29, v29\n" \
"v_exp_f32 v30, v30\n" \
"v_exp_f32 v31, v31\n" \
"s_nop 0\n" \
"v_cvt_pk_bf16_f32 v164, v24, v25\n" \
"v_cvt_pk_bf16_f32 v165, v26, v27\n" \
"v_cvt_pk_bf16_f32 v166, v28, v29\n" \
"v_cvt_pk_bf16_f32 v167, v30, v31\n" \
"s_nop 1\n" \
"v_mfma_f32_32x32x16_bf16 v[168:183], a[12:15], v[164:167], v[168:183]\n" \
"v_add_u16 v4, 0x1000, v4\n" \
"ds_read_b128 a[8:11], v4\n" \
"ds_read_b128 a[12:15], v4 offset:768\n" \
"s_waitcnt lgkmcnt(2)\n" \
"v_mfma_f32_32x32x16_bf16 v[16:31], a[0:3], v[216:219], v[184:199]\n" \
"v_add_u16 v3, 0x1000, v3\n" \
"v_mfma_f32_32x32x16_bf16 v[16:31], a[4:7], v[220:223], v[16:31]\n" \
"ds_read_b128 a[0:3], v3\n" \
"ds_read_b128 a[4:7], v3 offset:32\n" \
"v_fma_f32 v32, v32, s51, -v8\n" \
"v_fma_f32 v33, v33, s51, -v8\n" \
"v_fma_f32 v34, v34, s51, -v8\n" \
"v_fma_f32 v35, v35, s51, -v8\n" \
"v_fma_f32 v36, v36, s51, -v8\n" \
"v_fma_f32 v37, v37, s51, -v8\n" \
"v_fma_f32 v38, v38, s51, -v8\n" \
"v_fma_f32 v39, v39, s51, -v8\n" \
"v_exp_f32 v32, v32\n" \
"v_exp_f32 v33, v33\n" \
"v_exp_f32 v34, v34\n" \
"v_exp_f32 v35, v35\n" \
"v_exp_f32 v36, v36\n" \
"v_exp_f32 v37, v37\n" \
"v_exp_f32 v38, v38\n" \
"v_exp_f32 v39, v39\n" \
"v_fma_f32 v40, v40, s51, -v8\n" \
"v_cvt_pk_bf16_f32 v160, v32, v33\n" \
"v_cvt_pk_bf16_f32 v161, v34, v35\n" \
"v_cvt_pk_bf16_f32 v162, v36, v37\n" \
"v_cvt_pk_bf16_f32 v163, v38, v39\n" \
"v_fma_f32 v41, v41, s51, -v8\n" \
"v_fma_f32 v42, v42, s51, -v8\n" \
"s_waitcnt lgkmcnt(2)\n" \
"v_mfma_f32_32x32x16_bf16 v[168:183], a[8:11], v[160:163], v[168:183]\n" \
"v_fma_f32 v43, v43, s51, -v8\n" \
"v_fma_f32 v44, v44, s51, -v8\n" \
"v_fma_f32 v45, v45, s51, -v8\n" \
"v_fma_f32 v46, v46, s51, -v8\n" \
"v_fma_f32 v47, v47, s51, -v8\n" \
"s_mov_b32 exec_hi, 0\n" \
"global_load_lds_dwordx4 v5, s[40:41] offset:1024 sc1\n" \
"s_mov_b32 exec_hi, -1\n" \
"v_exp_f32 v40, v40\n" \
"v_exp_f32 v41, v41\n" \
"v_exp_f32 v42, v42\n" \
"v_exp_f32 v43, v43\n" \
"v_exp_f32 v44, v44\n" \
"v_exp_f32 v45, v45\n" \
"v_exp_f32 v46, v46\n" \
"v_exp_f32 v47, v47\n" \
"v_max3_f32 v10, v10, v16, v17\n" \
"v_cvt_pk_bf16_f32 v164, v40, v41\n" \
"v_cvt_pk_bf16_f32 v165, v42, v43\n" \
"v_cvt_pk_bf16_f32 v166, v44, v45\n" \
"v_cvt_pk_bf16_f32 v167, v46, v47\n" \
"v_max3_f32 v11, v11, v18, v19\n" \
"v_max3_f32 v10, v10, v20, v21\n" \
"v_mfma_f32_32x32x16_bf16 v[168:183], a[12:15], v[164:167], v[168:183]\n" \
"v_add_u16 v4, 0x1000, v4\n" \
"v_max3_f32 v11, v11, v22, v23\n" \
"v_max3_f32 v10, v10, v24, v25\n" \
"v_max3_f32 v11, v11, v26, v27\n" \
"v_max3_f32 v10, v10, v28, v29\n" \
"v_max3_f32 v11, v11, v30, v31\n" \
"ds_read_b128 a[8:11], v4\n" \
"ds_read_b128 a[12:15], v4 offset:768\n" \
"s_waitcnt lgkmcnt(2)\n" \
"v_mfma_f32_32x32x16_bf16 v[32:47], a[0:3], v[216:219], 0\n" \
"v_add_u16 v3, 0x1000, v3\n" \
"v_mfma_f32_32x32x16_bf16 v[32:47], a[4:7], v[220:223], v[32:47]\n" \
"ds_read_b128 a[0:3], v3\n" \
"ds_read_b128 a[4:7], v3 offset:32\n" \
"v_fma_f32 v48, v48, s51, -v8\n" \
"v_fma_f32 v49, v49, s51, -v8\n" \
"v_fma_f32 v50, v50, s51, -v8\n" \
"v_fma_f32 v51, v51, s51, -v8\n" \
"v_fma_f32 v52, v52, s51, -v8\n" \
"v_fma_f32 v53, v53, s51, -v8\n" \
"v_fma_f32 v54, v54, s51, -v8\n" \
"v_fma_f32 v55, v55, s51, -v8\n" \
"v_exp_f32 v48, v48\n" \
"v_exp_f32 v49, v49\n" \
"v_exp_f32 v50, v50\n" \
"v_exp_f32 v51, v51\n" \
"v_exp_f32 v52, v52\n" \
"v_exp_f32 v53, v53\n" \
"v_exp_f32 v54, v54\n" \
"v_exp_f32 v55, v55\n" \
"v_fma_f32 v56, v56, s51, -v8\n" \
"v_cvt_pk_bf16_f32 v160, v48, v49\n" \
"v_cvt_pk_bf16_f32 v161, v50, v51\n" \
"v_cvt_pk_bf16_f32 v162, v52, v53\n" \
"v_cvt_pk_bf16_f32 v163, v54, v55\n" \
"v_fma_f32 v57, v57, s51, -v8\n" \
"v_fma_f32 v58, v58, s51, -v8\n" \
"s_waitcnt lgkmcnt(2)\n" \
"v_mfma_f32_32x32x16_bf16 v[168:183], a[8:11], v[160:163], v[168:183]\n" \
"v_fma_f32 v59, v59, s51, -v8\n" \
"v_fma_f32 v60, v60, s51, -v8\n" \
"v_fma_f32 v61, v61, s51, -v8\n" \
"v_fma_f32 v62, v62, s51, -v8\n" \
"v_fma_f32 v63, v63, s51, -v8\n" \
"s_mov_b32 m0, s50\n" \
"s_nop 0\n" \
"global_load_lds_dwordx4 v5, s[42:43] sc1\n" \
"v_exp_f32 v56, v56\n" \
"v_exp_f32 v57, v57\n" \
"v_exp_f32 v58, v58\n" \
"v_exp_f32 v59, v59\n" \
"v_exp_f32 v60, v60\n" \
"v_exp_f32 v61, v61\n" \
"v_exp_f32 v62, v62\n" \
"v_exp_f32 v63, v63\n" \
"v_max3_f32 v10, v10, v32, v33\n" \
"v_cvt_pk_bf16_f32 v164, v56, v57\n" \
"v_cvt_pk_bf16_f32 v165, v58, v59\n" \
"v_cvt_pk_bf16_f32 v166, v60, v61\n" \
"v_cvt_pk_bf16_f32 v167, v62, v63\n" \
"v_max3_f32 v11, v11, v34, v35\n" \
"v_max3_f32 v10, v10, v36, v37\n" \
"v_mfma_f32_32x32x16_bf16 v[168:183], a[12:15], v[164:167], v[168:183]\n" \
"v_add_u16 v4, 0x1000, v4\n" \
"v_max3_f32 v11, v11, v38, v39\n" \
"v_max3_f32 v10, v10, v40, v41\n" \
"v_max3_f32 v11, v11, v42, v43\n" \
"v_max3_f32 v10, v10, v44, v45\n" \
"v_max3_f32 v11, v11, v46, v47\n" \
"ds_read_b128 a[8:11], v4\n" \
"ds_read_b128 a[12:15], v4 offset:768\n" \
"s_waitcnt lgkmcnt(2)\n" \
"v_mfma_f32_32x32x16_bf16 v[48:63], a[0:3], v[216:219], 0\n" \
"v_add_u16 v3, 0x1000, v3\n" \
"v_mfma_f32_32x32x16_bf16 v[48:63], a[4:7], v[220:223], v[48:63]\n" \
"ds_read_b128 a[0:3], v3\n" \
"ds_read_b128 a[4:7], v3 offset:32\n" \
"v_fma_f32 v64, v64, s51, -v8\n" \
"v_fma_f32 v65, v65, s51, -v8\n" \
"v_fma_f32 v66, v66, s51, -v8\n" \
"v_fma_f32 v67, v67, s51, -v8\n" \
"v_fma_f32 v68, v68, s51, -v8\n" \
"v_fma_f32 v69, v69, s51, -v8\n" \
"v_fma_f32 v70, v70, s51, -v8\n" \
"v_fma_f32 v71, v71, s51, -v8\n" \
"v_exp_f32 v64, v64\n" \
"v_exp_f32 v65, v65\n" \
"v_exp_f32 v66, v66\n" \
"v_exp_f32 v67, v67\n" \
"v_exp_f32 v68, v68\n" \
"v_exp_f32 v69, v69\n" \
"v_exp_f32 v70, v70\n" \
"v_exp_f32 v71, v71\n" \
"v_fma_f32 v72, v72, s51, -v8\n" \
"v_cvt_pk_bf16_f32 v160, v64, v65\n" \
"v_cvt_pk_bf16_f32 v161, v66, v67\n" \
"v_cvt_pk_bf16_f32 v162, v68, v69\n" \
"v_cvt_pk_bf16_f32 v163, v70, v71\n" \
"v_fma_f32 v73, v73, s51, -v8\n" \
"v_fma_f32 v74, v74, s51, -v8\n" \
"s_waitcnt lgkmcnt(2)\n" \
"v_mfma_f32_32x32x16_bf16 v[168:183], a[8:11], v[160:163], v[168:183]\n" \
"v_fma_f32 v75, v75, s51, -v8\n" \
"v_fma_f32 v76, v76, s51, -v8\n" \
"v_fma_f32 v77, v77, s51, -v8\n" \
"v_fma_f32 v78, v78, s51, -v8\n" \
"v_fma_f32 v79, v79, s51, -v8\n" \
"s_mov_b32 exec_hi, 0\n" \
"global_load_lds_dwordx4 v5, s[42:43] offset:1024 sc1\n" \
"s_mov_b32 exec_hi, -1\n" \
"v_exp_f32 v72, v72\n" \
"v_exp_f32 v73, v73\n" \
"v_exp_f32 v74, v74\n" \
"v_exp_f32 v75, v75\n" \
"v_exp_f32 v76, v76\n" \
"v_exp_f32 v77, v77\n" \
"v_exp_f32 v78, v78\n" \
"v_exp_f32 v79, v79\n" \
"v_max3_f32 v10, v10, v48, v49\n" \
"v_cvt_pk_bf16_f32 v164, v72, v73\n" \
"v_cvt_pk_bf16_f32 v165, v74, v75\n" \
"v_cvt_pk_bf16_f32 v166, v76, v77\n" \
"v_cvt_pk_bf16_f32 v167, v78, v79\n" \
"v_max3_f32 v11, v11, v50, v51\n" \
"v_max3_f32 v10, v10, v52, v53\n" \
"v_mfma_f32_32x32x16_bf16 v[168:183], a[12:15], v[164:167], v[168:183]\n" \
"v_add_u16 v4, 0x1000, v4\n" \
"v_max3_f32 v11, v11, v54, v55\n" \
"v_max3_f32 v10, v10, v56, v57\n" \
"v_max3_f32 v11, v11, v58, v59\n" \
"v_max3_f32 v10, v10, v60, v61\n" \
"v_max3_f32 v11, v11, v62, v63\n" \
"ds_read_b128 a[8:11], v4\n" \
"ds_read_b128 a[12:15], v4 offset:768\n" \
"s_waitcnt lgkmcnt(2)\n" \
"v_mfma_f32_32x32x16_bf16 v[64:79], a[0:3], v[216:219], 0\n" \
"v_add_u16 v3, 0x1000, v3\n" \
"v_mfma_f32_32x32x16_bf16 v[64:79], a[4:7], v[220:223], v[64:79]\n" \
"ds_read_b128 a[0:3], v3\n" \
"ds_read_b128 a[4:7], v3 offset:32\n" \
"v_fma_f32 v80, v80, s51, -v8\n" \
"v_fma_f32 v81, v81, s51, -v8\n" \
"v_fma_f32 v82, v82, s51, -v8\n" \
"v_fma_f32 v83, v83, s51, -v8\n" \
"v_fma_f32 v84, v84, s51, -v8\n" \
"v_fma_f32 v85, v85, s51, -v8\n" \
"v_fma_f32 v86, v86, s51, -v8\n" \
"v_fma_f32 v87, v87, s51, -v8\n" \
"v_exp_f32 v80, v80\n" \
"v_exp_f32 v81, v81\n" \
"v_exp_f32 v82, v82\n" \
"v_exp_f32 v83, v83\n" \
"v_exp_f32 v84, v84\n" \
"v_exp_f32 v85, v85\n" \
"v_exp_f32 v86, v86\n" \
"v_exp_f32 v87, v87\n" \
"v_fma_f32 v88, v88, s51, -v8\n" \
"v_cvt_pk_bf16_f32 v160, v80, v81\n" \
"v_cvt_pk_bf16_f32 v161, v82, v83\n" \
"v_cvt_pk_bf16_f32 v162, v84, v85\n" \
"v_cvt_pk_bf16_f32 v163, v86, v87\n" \
"v_fma_f32 v89, v89, s51, -v8\n" \
"v_fma_f32 v90, v90, s51, -v8\n" \
"s_waitcnt lgkmcnt(2)\n" \
"v_mfma_f32_32x32x16_bf16 v[168:183], a[8:11], v[160:163], v[168:183]\n" \
"v_fma_f32 v91, v91, s51, -v8\n" \
"v_fma_f32 v92, v92, s51, -v8\n" \
"v_fma_f32 v93, v93, s51, -v8\n" \
"v_fma_f32 v94, v94, s51, -v8\n" \
"v_fma_f32 v95, v95, s51, -v8\n" \
"global_load_dwordx4 v[224:227], v6, s[44:45] sc1\n" \
"v_exp_f32 v88, v88\n" \
"v_exp_f32 v89, v89\n" \
"v_exp_f32 v90, v90\n" \
"v_exp_f32 v91, v91\n" \
"v_exp_f32 v92, v92\n" \
"v_exp_f32 v93, v93\n" \
"v_exp_f32 v94, v94\n" \
"v_exp_f32 v95, v95\n" \
"v_max3_f32 v10, v10, v64, v65\n" \
"v_cvt_pk_bf16_f32 v164, v88, v89\n" \
"v_cvt_pk_bf16_f32 v165, v90, v91\n" \
"v_cvt_pk_bf16_f32 v166, v92, v93\n" \
"v_cvt_pk_bf16_f32 v167, v94, v95\n" \
"v_max3_f32 v11, v11, v66, v67\n" \
"v_max3_f32 v10, v10, v68, v69\n" \
"v_mfma_f32_32x32x16_bf16 v[168:183], a[12:15], v[164:167], v[168:183]\n" \
"v_add_u16 v4, 0x1000, v4\n" \
"v_max3_f32 v11, v11, v70, v71\n" \
"v_max3_f32 v10, v10, v72, v73\n" \
"v_max3_f32 v11, v11, v74, v75\n" \
"v_max3_f32 v10, v10, v76, v77\n" \
"v_max3_f32 v11, v11, v78, v79\n" \
"ds_read_b128 a[8:11], v4\n" \
"ds_read_b128 a[12:15], v4 offset:768\n" \
"s_waitcnt lgkmcnt(2)\n" \
"v_mfma_f32_32x32x16_bf16 v[80:95], a[0:3], v[216:219], 0\n" \
"v_add_u16 v3, 0x1000, v3\n" \
"v_mfma_f32_32x32x16_bf16 v[80:95], a[4:7], v[220:223], v[80:95]\n" \
"ds_read_b128 a[0:3], v3\n" \
"ds_read_b128 a[4:7], v3 offset:32\n" \
"v_fma_f32 v96, v96, s51, -v8\n" \
"v_fma_f32 v97, v97, s51, -v8\n" \
"v_fma_f32 v98, v98, s51, -v8\n" \
"v_fma_f32 v99, v99, s51, -v8\n" \
"v_fma_f32 v100, v100, s51, -v8\n" \
"v_fma_f32 v101, v101, s51, -v8\n" \
"v_fma_f32 v102, v102, s51, -v8\n" \
"v_fma_f32 v103, v103, s51, -v8\n" \
"v_exp_f32 v96, v96\n" \
"v_exp_f32 v97, v97\n" \
"v_exp_f32 v98, v98\n" \
"v_exp_f32 v99, v99\n" \
"v_exp_f32 v100, v100\n" \
"v_exp_f32 v101, v101\n" \
"v_exp_f32 v102, v102\n" \
"v_exp_f32 v103, v103\n" \
"v_fma_f32 v104, v104, s51, -v8\n" \
"v_cvt_pk_bf16_f32 v160, v96, v97\n" \
"v_cvt_pk_bf16_f32 v161, v98, v99\n" \
"v_cvt_pk_bf16_f32 v162, v100, v101\n" \
"v_cvt_pk_bf16_f32 v163, v102, v103\n" \
"v_fma_f32 v105, v105, s51, -v8\n" \
"v_fma_f32 v106, v106, s51, -v8\n" \
"s_waitcnt lgkmcnt(2)\n" \
"v_mfma_f32_32x32x16_bf16 v[168:183], a[8:11], v[160:163], v[168:183]\n" \
"v_fma_f32 v107, v107, s51, -v8\n" \
"v_fma_f32 v108, v108, s51, -v8\n" \
"v_fma_f32 v109, v109, s51, -v8\n" \
"v_fma_f32 v110, v110, s51, -v8\n" \
"v_fma_f32 v111, v111, s51, -v8\n" \
"s_mov_b32 exec_hi, 0\n" \
"global_load_dwordx4 v[228:231], v6, s[44:45] offset:32 sc1\n" \
"s_mov_b32 exec_hi, -1\n" \
"v_exp_f32 v104, v104\n" \
"v_exp_f32 v105, v105\n" \
"v_exp_f32 v106, v106\n" \
"v_exp_f32 v107, v107\n" \
"v_exp_f32 v108, v108\n" \
"v_exp_f32 v109, v109\n" \
"v_exp_f32 v110, v110\n" \
"v_exp_f32 v111, v111\n" \
"v_max3_f32 v10, v10, v80, v81\n" \
"v_cvt_pk_bf16_f32 v164, v104, v105\n" \
"v_cvt_pk_bf16_f32 v165, v106, v107\n" \
"v_cvt_pk_bf16_f32 v166, v108, v109\n" \
"v_cvt_pk_bf16_f32 v167, v110, v111\n" \
"v_max3_f32 v11, v11, v82, v83\n" \
"v_max3_f32 v10, v10, v84, v85\n" \
"v_mfma_f32_32x32x16_bf16 v[168:183], a[12:15], v[164:167], v[168:183]\n" \
"v_add_u16 v4, 0x1000, v4\n" \
"v_max3_f32 v11, v11, v86, v87\n" \
"v_max3_f32 v10, v10, v88, v89\n" \
"v_max3_f32 v11, v11, v90, v91\n" \
"v_max3_f32 v10, v10, v92, v93\n" \
"v_max3_f32 v11, v11, v94, v95\n" \
"ds_read_b128 a[8:11], v4\n" \
"ds_read_b128 a[12:15], v4 offset:768\n" \
"s_waitcnt lgkmcnt(2)\n" \
"v_mfma_f32_32x32x16_bf16 v[96:111], a[0:3], v[216:219], 0\n" \
"v_add_u16 v3, 0x1000, v3\n" \
"v_mfma_f32_32x32x16_bf16 v[96:111], a[4:7], v[220:223], v[96:111]\n" \
"ds_read_b128 a[0:3], v3\n" \
"ds_read_b128 a[4:7], v3 offset:32\n" \
"v_fma_f32 v112, v112, s51, -v8\n" \
"v_fma_f32 v113, v113, s51, -v8\n" \
"v_fma_f32 v114, v114, s51, -v8\n" \
"v_fma_f32 v115, v115, s51, -v8\n" \
"v_fma_f32 v116, v116, s51, -v8\n" \
"v_fma_f32 v117, v117, s51, -v8\n" \
"v_fma_f32 v118, v118, s51, -v8\n" \
"v_fma_f32 v119, v119, s51, -v8\n" \
"v_exp_f32 v112, v112\n" \
"v_exp_f32 v113, v113\n" \
"v_exp_f32 v114, v114\n" \
"v_exp_f32 v115, v115\n" \
"v_exp_f32 v116, v116\n" \
"v_exp_f32 v117, v117\n" \
"v_exp_f32 v118, v118\n" \
"v_exp_f32 v119, v119\n" \
"v_fma_f32 v120, v120, s51, -v8\n" \
"v_cvt_pk_bf16_f32 v160, v112, v113\n" \
"v_cvt_pk_bf16_f32 v161, v114, v115\n" \
"v_cvt_pk_bf16_f32 v162, v116, v117\n" \
"v_cvt_pk_bf16_f32 v163, v118, v119\n" \
"v_fma_f32 v121, v121, s51, -v8\n" \
"v_fma_f32 v122, v122, s51, -v8\n" \
"s_waitcnt lgkmcnt(2)\n" \
"v_mfma_f32_32x32x16_bf16 v[168:183], a[8:11], v[160:163], v[168:183]\n" \
"v_fma_f32 v123, v123, s51, -v8\n" \
"v_fma_f32 v124, v124, s51, -v8\n" \
"v_fma_f32 v125, v125, s51, -v8\n" \
"v_fma_f32 v126, v126, s51, -v8\n" \
"v_fma_f32 v127, v127, s51, -v8\n" \
"s_add_u32 s40, s40, 6144\n" \
"s_addc_u32 s41, s41, 0\n" \
"s_add_u32 s42, s42, 6144\n" \
"s_addc_u32 s43, s43, 0\n" \
"s_add_u32 s44, s44, 6144\n" \
"s_addc_u32 s45, s45, 0\n" \
"v_exp_f32 v120, v120\n" \
"v_exp_f32 v121, v121\n" \
"v_exp_f32 v122, v122\n" \
"v_exp_f32 v123, v123\n" \
"v_exp_f32 v124, v124\n" \
"v_exp_f32 v125, v125\n" \
"v_exp_f32 v126, v126\n" \
"v_exp_f32 v127, v127\n" \
"v_max3_f32 v10, v10, v96, v97\n" \
"v_cvt_pk_bf16_f32 v164, v120, v121\n" \
"v_cvt_pk_bf16_f32 v165, v122, v123\n" \
"v_cvt_pk_bf16_f32 v166, v124, v125\n" \
"v_cvt_pk_bf16_f32 v167, v126, v127\n" \
"v_max3_f32 v11, v11, v98, v99\n" \
"v_max3_f32 v10, v10, v100, v101\n" \
"v_mfma_f32_32x32x16_bf16 v[168:183], a[12:15], v[164:167], v[168:183]\n" \
"v_add_u16 v4, 0x1000, v4\n" \
"v_max3_f32 v11, v11, v102, v103\n" \
"v_max3_f32 v10, v10, v104, v105\n" \
"v_max3_f32 v11, v11, v106, v107\n" \
"v_max3_f32 v10, v10, v108, v109\n" \
"v_max3_f32 v11, v11, v110, v111\n" \
"ds_read_b128 a[8:11], v4\n" \
"ds_read_b128 a[12:15], v4 offset:768\n" \
"s_waitcnt lgkmcnt(2)\n" \
"v_mfma_f32_32x32x16_bf16 v[112:127], a[0:3], v[216:219], 0\n" \
"v_add_u16 v3, 0x1000, v3\n" \
"v_mfma_f32_32x32x16_bf16 v[112:127], a[4:7], v[220:223], v[112:127]\n" \
"ds_read_b128 a[0:3], v3\n" \
"ds_read_b128 a[4:7], v3 offset:32\n" \
"v_fma_f32 v128, v128, s51, -v8\n" \
"v_fma_f32 v129, v129, s51, -v8\n" \
"v_fma_f32 v130, v130, s51, -v8\n" \
"v_fma_f32 v131, v131, s51, -v8\n" \
"v_fma_f32 v132, v132, s51, -v8\n" \
"v_fma_f32 v133, v133, s51, -v8\n" \
"v_fma_f32 v134, v134, s51, -v8\n" \
"v_fma_f32 v135, v135, s51, -v8\n" \
"v_exp_f32 v128, v128\n" \
"v_exp_f32 v129, v129\n" \
"v_exp_f32 v130, v130\n" \
"v_exp_f32 v131, v131\n" \
"v_exp_f32 v132, v132\n" \
"v_exp_f32 v133, v133\n" \
"v_exp_f32 v134, v134\n" \
"v_exp_f32 v135, v135\n" \
"v_fma_f32 v136, v136, s51, -v8\n" \
"v_cvt_pk_bf16_f32 v160, v128, v129\n" \
"v_cvt_pk_bf16_f32 v161, v130, v131\n" \
"v_cvt_pk_bf16_f32 v162, v132, v133\n" \
"v_cvt_pk_bf16_f32 v163, v134, v135\n" \
"v_fma_f32 v137, v137, s51, -v8\n" \
"v_fma_f32 v138, v138, s51, -v8\n" \
"s_waitcnt lgkmcnt(2)\n" \
"v_mfma_f32_32x32x16_bf16 v[168:183], a[8:11], v[160:163], v[168:183]\n" \
"v_fma_f32 v139, v139, s51, -v8\n" \
"v_fma_f32 v140, v140, s51, -v8\n" \
"v_fma_f32 v141, v141, s51, -v8\n" \
"v_fma_f32 v142, v142, s51, -v8\n" \
"v_fma_f32 v143, v143, s51, -v8\n" \
"s_add_u32 s49, s49, 16384\n" \
"s_and_b32 s49, s49, 0xffff\n" \
"s_add_u32 s50, s50, 16384\n" \
"s_and_b32 s50, s50, 0xffff\n" \
"v_exp_f32 v136, v136\n" \
"v_exp_f32 v137, v137\n" \
"v_exp_f32 v138, v138\n" \
"v_exp_f32 v139, v139\n" \
"v_exp_f32 v140, v140\n" \
"v_exp_f32 v141, v141\n" \
"v_exp_f32 v142, v142\n" \
"v_exp_f32 v143, v143\n" \
"v_max3_f32 v10, v10, v112, v113\n" \
"v_cvt_pk_bf16_f32 v164, v136, v137\n" \
"v_cvt_pk_bf16_f32 v165, v138, v139\n" \
"v_cvt_pk_bf16_f32 v166, v140, v141\n" \
"v_cvt_pk_bf16_f32 v167, v142, v143\n" \
"v_max3_f32 v11, v11, v114, v115\n" \
"v_max3_f32 v10, v10, v116, v117\n" \
"v_mfma_f32_32x32x16_bf16 v[168:183], a[12:15], v[164:167], v[168:183]\n" \
"v_add_u16 v4, 0x1000, v4\n" \
"v_max3_f32 v11, v11, v118, v119\n" \
"v_max3_f32 v10, v10, v120, v121\n" \
"v_max3_f32 v11, v11, v122, v123\n" \
"v_max3_f32 v10, v10, v124, v125\n" \
"v_max3_f32 v11, v11, v126, v127\n" \
"ds_read_b128 a[8:11], v4\n" \
"ds_read_b128 a[12:15], v4 offset:768\n" \
"s_waitcnt lgkmcnt(2)\n" \
"v_mfma_f32_32x32x16_bf16 v[128:143], a[0:3], v[216:219], 0\n" \
"v_add_u16 v3, 0x1000, v3\n" \
"v_mfma_f32_32x32x16_bf16 v[128:143], a[4:7], v[220:223], v[128:143]\n" \
"ds_read_b128 a[0:3], v3\n" \
"ds_read_b128 a[4:7], v3 offset:32\n" \
"v_fma_f32 v144, v144, s51, -v8\n" \
"v_fma_f32 v145, v145, s51, -v8\n" \
"v_fma_f32 v146, v146, s51, -v8\n" \
"v_fma_f32 v147, v147, s51, -v8\n" \
"v_fma_f32 v148, v148, s51, -v8\n" \
"v_fma_f32 v149, v149, s51, -v8\n" \
"v_fma_f32 v150, v150, s51, -v8\n" \
"v_fma_f32 v151, v151, s51, -v8\n" \
"v_exp_f32 v144, v144\n" \
"v_exp_f32 v145, v145\n" \
"v_exp_f32 v146, v146\n" \
"v_exp_f32 v147, v147\n" \
"v_exp_f32 v148, v148\n" \
"v_exp_f32 v149, v149\n" \
"v_exp_f32 v150, v150\n" \
"v_exp_f32 v151, v151\n" \
"v_fma_f32 v152, v152, s51, -v8\n" \
"v_cvt_pk_bf16_f32 v160, v144, v145\n" \
"v_cvt_pk_bf16_f32 v161, v146, v147\n" \
"v_cvt_pk_bf16_f32 v162, v148, v149\n" \
"v_cvt_pk_bf16_f32 v163, v150, v151\n" \
"v_fma_f32 v153, v153, s51, -v8\n" \
"v_fma_f32 v154, v154, s51, -v8\n" \
"s_waitcnt lgkmcnt(2)\n" \
"v_mfma_f32_32x32x16_bf16 v[168:183], a[8:11], v[160:163], v[168:183]\n" \
"v_fma_f32 v155, v155, s51, -v8\n" \
"v_fma_f32 v156, v156, s51, -v8\n" \
"v_fma_f32 v157, v157, s51, -v8\n" \
"v_fma_f32 v158, v158, s51, -v8\n" \
"v_fma_f32 v159, v159, s51, -v8\n" \
"v_exp_f32 v152, v152\n" \
"v_exp_f32 v153, v153\n" \
"v_exp_f32 v154, v154\n" \
"v_exp_f32 v155, v155\n" \
"v_exp_f32 v156, v156\n" \
"v_exp_f32 v157, v157\n" \
"v_exp_f32 v158, v158\n" \
"v_exp_f32 v159, v159\n" \
"v_max3_f32 v10, v10, v128, v129\n" \
"v_cvt_pk_bf16_f32 v164, v152, v153\n" \
"v_cvt_pk_bf16_f32 v165, v154, v155\n" \
"v_cvt_pk_bf16_f32 v166, v156, v157\n" \
"v_cvt_pk_bf16_f32 v167, v158, v159\n" \
"v_max3_f32 v11, v11, v130, v131\n" \
"v_max3_f32 v10, v10, v132, v133\n" \
"v_mfma_f32_32x32x16_bf16 v[168:183], a[12:15], v[164:167], v[168:183]\n" \
"v_add_u16 v4, 0xc000, v4\n" \
"v_max3_f32 v11, v11, v134, v135\n" \
"v_max3_f32 v10, v10, v136, v137\n" \
"v_max3_f32 v11, v11, v138, v139\n" \
"v_max3_f32 v10, v10, v140, v141\n" \
"v_max3_f32 v11, v11, v142, v143\n" \
"ds_read_b128 a[8:11], v4\n" \
"ds_read_b128 a[12:15], v4 offset:768\n" \
"s_waitcnt lgkmcnt(2)\n" \
"v_mfma_f32_32x32x16_bf16 v[144:159], a[0:3], v[216:219], v[200:215]\n" \
"v_add_u16 v3, 0xc000, v3\n" \
"v_mfma_f32_32x32x16_bf16 v[144:159], a[4:7], v[220:223], v[144:159]\n" \
"ds_read_b128 a[0:3], v3\n" \
"ds_read_b128 a[4:7], v3 offset:32\n" \
"v_mov_b32 v232, v176\n" \
"v_mov_b32 v233, v176\n" \
"s_cmp_lt_i32 s48, 0\n" \
"s_nop 0\n" \
"v_permlane32_swap_b32 v232, v233\n" \
"s_nop 0\n" \
"v_rcp_f32 v233, v233\n" \
"s_cbranch_scc1 La_first%=\n" \
"v_mul_f32 v168, v168, v233\n" \
"v_mul_f32 v169, v169, v233\n" \
"v_mul_f32 v170, v170, v233\n" \
"v_mul_f32 v171, v171, v233\n" \
"v_mul_f32 v172, v172, v233\n" \
"v_mul_f32 v173, v173, v233\n" \
"v_mul_f32 v174, v174, v233\n" \
"v_mul_f32 v175, v175, v233\n" \
"v_mul_f32 v176, v176, v233\n" \
"v_mul_f32 v177, v177, v233\n" \
"v_mul_f32 v178, v178, v233\n" \
"v_mul_f32 v179, v179, v233\n" \
"v_cvt_pk_bf16_f32 v234, v168, v169\n" \
"v_cvt_pk_bf16_f32 v235, v170, v171\n" \
"v_cvt_pk_bf16_f32 v236, v172, v173\n" \
"v_cvt_pk_bf16_f32 v237, v174, v175\n" \
"v_cvt_pk_bf16_f32 v238, v176, v177\n" \
"v_cvt_pk_bf16_f32 v239, v178, v179\n" \
"global_store_dwordx2 v7, v[234:235], s[46:47] sc1\n" \
"global_store_dwordx2 v7, v[236:237], s[46:47] offset:16 sc1\n" \
"s_mov_b32 exec_hi, 0\n" \
"global_store_dwordx2 v7, v[238:239], s[46:47] offset:32 sc1\n" \
"s_mov_b32 exec_hi, -1\n" \
"s_add_u32 s46, s46, 163840\n" \
"s_addc_u32 s47, s47, 0\n" \
"La_next%=:\n" \
"v_max3_f32 v10, v10, v144, v145\n" \
"v_max3_f32 v11, v11, v146, v147\n" \
"v_max3_f32 v10, v10, v148, v149\n" \
"v_max3_f32 v11, v11, v150, v151\n" \
"v_max3_f32 v10, v10, v152, v153\n" \
"v_max3_f32 v11, v11, v154, v155\n" \
"v_max3_f32 v10, v10, v156, v157\n" \
"v_max3_f32 v11, v11, v158, v159\n" \
"v_max_f32 v10, v10, v11\n" \
"s_add_u32 s48, s48, 1\n" \
"v_mov_b32 v11, v10\n" \
"s_cmp_lt_i32 s48, 16\n" \
"s_nop 0\n" \
"v_permlane32_swap_b32 v10, v11\n" \
"s_nop 0\n" \
"v_max_f32 v10, v10, v11\n" \
"v_mul_f32 v8, s51, v10\n" \
"s_cbranch_scc1 La_loop%=\n" \
"s_branch La_done%=\n" \
"La_first%=:\n" \
"s_waitcnt vmcnt(0)\n" \
"s_nop 7\n" \
"s_branch La_next%=\n" \
"La_done%=:\n" \
"s_waitcnt vmcnt(0) lgkmcnt(0)\n" \
""
#define A2_CLOB "v0", "v1", "v2", "v3", "v4", "v5", "v6", "v7", "v8", "v9", "v10", "v11", "v12", "v13", "v14", "v15", "v16", "v17", "v18", "v19", "v20", "v21", "v22", "v23", "v24", "v25", "v26", "v27", "v28", "v29", "v30", "v31", "v32", "v33", "v34", "v35", "v36", "v37", "v38", "v39", "v40", "v41", "v42", "v43", "v44", "v45", "v46", "v47", "v48", "v49", "v50", "v51", "v52", "v53", "v54", "v55", "v56", "v57", "v58", "v59", "v60", "v61", "v62", "v63", "v64", "v65", "v66", "v67", "v68", "v69", "v70", "v71", "v72", "v73", "v74", "v75", "v76", "v77", "v78", "v79", "v80", "v81", "v82", "v83", "v84", "v85", "v86", "v87", "v88", "v89", "v90", "v91", "v92", "v93", "v94", "v95", "v96", "v97", "v98", "v99", "v100", "v101", "v102", "v103", "v104", "v105", "v106", "v107", "v108", "v109", "v110", "v111", "v112", "v113", "v114", "v115", "v116", "v117", "v118", "v119", "v120", "v121", "v122", "v123", "v124", "v125", "v126", "v127", "v128", "v129", "v130", "v131", "v132", "v133", "v134", "v135", "v136", "v137", "v138", "v139", "v140", "v141", "v142", "v143", "v144", "v145", "v146", "v147", "v148", "v149", "v150", "v151", "v152", "v153", "v154", "v155", "v156", "v157", "v158", "v159", "v160", "v161", "v162", "v163", "v164", "v165", "v166", "v167", "v168", "v169", "v170", "v171", "v172", "v173", "v174", "v175", "v176", "v177", "v178", "v179", "v180", "v181", "v182", "v183", "v184", "v185", "v186", "v187", "v188", "v189", "v190", "v191", "v192", "v193", "v194", "v195", "v196", "v197", "v198", "v199", "v200", "v201", "v202", "v203", "v204", "v205", "v206", "v207", "v208", "v209", "v210", "v211", "v212", "v213", "v214", "v215", "v216", "v217", "v218", "v219", "v220", "v221", "v222", "v223", "v224", "v225", "v226", "v227", "v228", "v229", "v230", "v231", "v232", "v233", "v234", "v235", "v236", "v237", "v238", "v239", "a0", "a1", "a2", "a3", "a4", "a5", "a6", "a7", "a8", "a9", "a10", "a11", "a12", "a13", "a14", "a15", "s40", "s41", "s42", "s43", "s44", "s45", "s46", "s47", "s48", "s49", "s50", "s51", "s52", "s53", "vcc", "scc", "memory"
#define T2_ASM \
"s_mov_b64 s[40:41], %0\n" \
"s_mov_b64 s[42:43], %1\n" \
"s_mov_b64 s[44:45], %2\n" \
"s_mov_b64 s[46:47], %3\n" \
"s_mov_b64 s[48:49], %4\n" \
"s_mov_b64 s[50:51], %5\n" \
"s_mov_b64 s[54:55], %6\n" \
"s_mov_b32 s56, %10\n" \
"s_add_u32 s52, s50, 0x20000\n" \
"s_addc_u32 s53, s51, 0\n" \
"s_mov_b32 s57, 0\n" \
"s_mov_b32 s58, 0\n" \
"s_mov_b32 s59, 0x3e0293ee\n" \
"v_mbcnt_lo_u32_b32 v8, -1, 0\n" \
"v_mbcnt_hi_u32_b32 v8, -1, v8\n" \
"v_lshlrev_b32 v0, 4, v8\n" \
"v_add_u32 v1, 4096, v0\n" \
"v_lshrrev_b32 v9, 4, v8\n" \
"v_and_b32 v10, 15, v8\n" \
"v_and_b32 v11, 7, v8\n" \
"v_lshl_add_u32 v11, v9, 3, v11\n" \
"v_lshlrev_b32 v2, 4, v11\n" \
"v_and_b32 v11, 1, v9\n" \
"v_lshl_add_u32 v11, v11, 4, v10\n" \
"v_lshlrev_b32 v3, 3, v11\n" \
"v_min_u32 v11, 11, v8\n" \
"v_lshlrev_b32 v11, 2, v11\n" \
"v_add_u32 v4, %7, v11\n" \
"v_and_b32 v11, 1, v8\n" \
"v_lshlrev_b32 v11, 4, v11\n" \
"v_add_u32 v5, %8, v11\n" \
"v_mov_b32 v15, %9\n" \
"v_mov_b32 v14, 0xff800000\n" \
"v_lshlrev_b32 v11, 2, v9\n" \
"v_sub_u32 v11, v10, v11\n" \
"v_cmp_le_i32 vcc, 0, v11\n" \
"s_nop 1\n" \
"v_cndmask_b32_e64 v16, v14, 0, vcc\n" \
"v_cmp_le_i32 vcc, 1, v11\n" \
"s_nop 1\n" \
"v_cndmask_b32_e64 v17, v14, 0, vcc\n" \
"v_cmp_le_i32 vcc, 2, v11\n" \
"s_nop 1\n" \
"v_cndmask_b32_e64 v18, v14, 0, vcc\n" \
"v_cmp_le_i32 vcc, 3, v11\n" \
"s_nop 1\n" \
"v_cndmask_b32_e64 v19, v14, 0, vcc\n" \
"v_cmp_gt_u32 vcc, 2, v9\n" \
"s_nop 1\n" \
"v_cndmask_b32_e64 v20, v14, 0, vcc\n" \
"v_mov_b32 v24, 0x3f803f80\n" \
"v_cndmask_b32_e64 v21, v14, 0, vcc\n" \
"v_mov_b32 v25, 0x3f803f80\n" \
"v_cndmask_b32_e64 v22, v14, 0, vcc\n" \
"v_mov_b32 v26, 0x3f803f80\n" \
"v_cndmask_b32_e64 v23, v14, 0, vcc\n" \
"v_mov_b32 v27, 0x3f803f80\n" \
"v_mov_b32 v42, 0\n" \
"v_mov_b32 v43, 0\n" \
"v_mov_b32 v13, 0\n" \
"s_add_u32 s61, s56, 1\n" \
"s_mov_b32 s60, 0\n" \
"Lt_p0%=:\n" \
"global_load_dword v6, v4, s[54:55] sc1\n" \
"s_add_u32 s60, s60, 1\n" \
"s_waitcnt vmcnt(0)\n" \
"v_cmp_gt_u32 vcc, s61, v6\n" \
"s_cbranch_vccz Lt_p0_ok%=\n" \
"s_sleep 4\n" \
"s_cmp_lt_u32 s60, 262144\n" \
"s_cbranch_scc1 Lt_p0%=\n" \
"s_branch Lt_fail%=\n" \
"Lt_p0_ok%=:\n" \
"s_nop 1\n" \
"v_min_u32_dpp v6, v6, v6 row_ror:8 row_mask:0xf bank_mask:0xf\n" \
"s_nop 1\n" \
"v_min_u32_dpp v6, v6, v6 row_ror:4 row_mask:0xf bank_mask:0xf\n" \
"s_nop 1\n" \
"v_min_u32_dpp v6, v6, v6 row_ror:2 row_mask:0xf bank_mask:0xf\n" \
"s_nop 1\n" \
"v_min_u32_dpp v6, v6, v6 row_ror:1 row_mask:0xf bank_mask:0xf\n" \
"s_nop 1\n" \
"v_readfirstlane_b32 s57, v6\n" \
"global_load_dwordx4 a[32:35], v0, s[42:43] offset:0 sc1\n" \
"global_load_dwordx4 a[0:3], v0, s[40:41] offset:0 sc1\n" \
"global_load_dwordx4 a[36:39], v0, s[42:43] offset:1024 sc1\n" \
"global_load_dwordx4 a[4:7], v0, s[40:41] offset:1024 sc1\n" \
"global_load_dwordx4 a[40:43], v0, s[42:43] offset:2048 sc1\n" \
"global_load_dwordx4 a[8:11], v0, s[40:41] offset:2048 sc1\n" \
"global_load_dwordx4 a[44:47], v0, s[42:43] offset:3072 sc1\n" \
"global_load_dwordx4 a[12:15], v0, s[40:41] offset:3072 sc1\n" \
"global_load_dwordx4 a[48:51], v1, s[42:43] offset:0 sc1\n" \
"global_load_dwordx4 a[16:19], v1, s[40:41] offset:0 sc1\n" \
"global_load_dwordx4 a[52:55], v1, s[42:43] offset:1024 sc1\n" \
"global_load_dwordx4 a[20:23], v1, s[40:41] offset:1024 sc1\n" \
"global_load_dwordx4 a[56:59], v1, s[42:43] offset:2048 sc1\n" \
"global_load_dwordx4 a[24:27], v1, s[40:41] offset:2048 sc1\n" \
"global_load_dwordx4 a[60:63], v1, s[42:43] offset:3072 sc1\n" \
"global_load_dwordx4 a[28:31], v1, s[40:41] offset:3072 sc1\n" \
"global_load_dwordx4 a[64:67], v0, s[44:45] offset:0 sc1\n" \
"global_load_dwordx4 a[68:71], v0, s[44:45] offset:1024 sc1\n" \
"global_load_dwordx4 a[72:75], v0, s[44:45] offset:2048 sc1\n" \
"global_load_dwordx4 a[76:79], v0, s[44:45] offset:3072 sc1\n" \
"global_load_dwordx4 a[80:83], v1, s[44:45] offset:0 sc1\n" \
"global_load_dwordx4 a[84:87], v1, s[44:45] offset:1024 sc1\n" \
"global_load_dwordx4 a[88:91], v1, s[44:45] offset:2048 sc1\n" \
"global_load_dwordx4 a[92:95], v1, s[44:45] offset:3072 sc1\n" \
"s_add_u32 s40, s40, 0x80000\n" \
"s_addc_u32 s41, s41, 0\n" \
"s_add_u32 s42, s42, 0x80000\n" \
"s_addc_u32 s43, s43, 0\n" \
"s_add_u32 s44, s44, 0x80000\n" \
"s_addc_u32 s45, s45, 0\n" \
"s_waitcnt vmcnt(0)\n" \
"Lt0_top%=:\n" \
"global_load_dword v7, v5, s[54:55] sc1\n" \
"s_add_u32 s61, s56, 2\n" \
"s_cmp_ge_u32 s61, 64\n" \
"s_cbranch_scc1 Lt0_nopf%=\n" \
"s_cmp_gt_u32 s57, s61\n" \
"s_cbranch_scc0 Lt0_t1%=\n" \
"Lt0_t1ok%=:\n" \
"global_load_dwordx4 a[128:131], v0, s[42:43] offset:0 sc1\n" \
"global_load_dwordx4 a[96:99], v0, s[40:41] offset:0 sc1\n" \
"global_load_dwordx4 a[132:135], v0, s[42:43] offset:1024 sc1\n" \
"global_load_dwordx4 a[100:103], v0, s[40:41] offset:1024 sc1\n" \
"global_load_dwordx4 a[136:139], v0, s[42:43] offset:2048 sc1\n" \
"global_load_dwordx4 a[104:107], v0, s[40:41] offset:2048 sc1\n" \
"global_load_dwordx4 a[140:143], v0, s[42:43] offset:3072 sc1\n" \
"global_load_dwordx4 a[108:111], v0, s[40:41] offset:3072 sc1\n" \
"global_load_dwordx4 a[144:147], v1, s[42:43] offset:0 sc1\n" \
"global_load_dwordx4 a[112:115], v1, s[40:41] offset:0 sc1\n" \
"global_load_dwordx4 a[148:151], v1, s[42:43] offset:1024 sc1\n" \
"global_load_dwordx4 a[116:119], v1, s[40:41] offset:1024 sc1\n" \
"global_load_dwordx4 a[152:155], v1, s[42:43] offset:2048 sc1\n" \
"global_load_dwordx4 a[120:123], v1, s[40:41] offset:2048 sc1\n" \
"global_load_dwordx4 a[156:159], v1, s[42:43] offset:3072 sc1\n" \
"global_load_dwordx4 a[124:127], v1, s[40:41] offset:3072 sc1\n" \
"global_load_dwordx4 a[160:163], v0, s[44:45] offset:0 sc1\n" \
"global_load_dwordx4 a[164:167], v0, s[44:45] offset:1024 sc1\n" \
"global_load_dwordx4 a[168:171], v0, s[44:45] offset:2048 sc1\n" \
"global_load_dwordx4 a[172:175], v0, s[44:45] offset:3072 sc1\n" \
"global_load_dwordx4 a[176:179], v1, s[44:45] offset:0 sc1\n" \
"global_load_dwordx4 a[180:183], v1, s[44:45] offset:1024 sc1\n" \
"global_load_dwordx4 a[184:187], v1, s[44:45] offset:2048 sc1\n" \
"global_load_dwordx4 a[188:191], v1, s[44:45] offset:3072 sc1\n" \
"s_add_u32 s40, s40, 0x80000\n" \
"s_addc_u32 s41, s41, 0\n" \
"s_add_u32 s42, s42, 0x80000\n" \
"s_addc_u32 s43, s43, 0\n" \
"s_add_u32 s44, s44, 0x80000\n" \
"s_addc_u32 s45, s45, 0\n" \
"Lt0_nopf%=:\n" \
"v_mfma_f32_16x16x32_bf16 v[28:31], a[32:35], a[0:3], v[16:19]\n" \
"v_mfma_f32_16x16x32_bf16 v[32:35], a[32:35], a[16:19], 0\n" \
"v_mfma_f32_16x16x32_bf16 v[36:39], a[48:51], a[16:19], v[16:19]\n" \
"v_mfma_f32_16x16x32_bf16 v[28:31], a[36:39], a[4:7], v[28:31]\n" \
"v_mfma_f32_16x16x32_bf16 v[32:35], a[36:39], a[20:23], v[32:35]\n" \
"v_mfma_f32_16x16x32_bf16 v[36:39], a[52:55], a[20:23], v[36:39]\n" \
"v_mfma_f32_16x16x32_bf16 v[28:31], a[40:43], a[8:11], v[28:31]\n" \
"v_mfma_f32_16x16x32_bf16 v[32:35], a[40:43], a[24:27], v[32:35]\n" \
"v_mfma_f32_16x16x32_bf16 v[36:39], a[56:59], a[24:27], v[36:39]\n" \
"v_mfma_f32_16x16x32_bf16 v[28:31], a[44:47], a[12:15], v[28:31]\n" \
"v_mfma_f32_16x16x32_bf16 v[32:35], a[44:47], a[28:31], v[32:35]\n" \
"v_mfma_f32_16x16x32_bf16 v[36:39], a[60:63], a[28:31], v[36:39]\n" \
"s_nop 0\n" \
"v_max3_f32 v196, v28, v29, v30\n" \
"v_max_f32 v196, v196, v31\n" \
"s_nop 1\n" \
"v_max3_f32 v197, v32, v33, v34\n" \
"s_nop 3\n" \
"v_max3_f32 v197, v197, v35, v36\n" \
"v_max3_f32 v197, v197, v37, v38\n" \
"v_max_f32 v197, v197, v39\n" \
"v_mov_b32 v198, v196\n" \
"v_mov_b32 v199, v197\n" \
"s_nop 0\n" \
"v_permlane32_swap_b32 v196, v198\n" \
"v_permlane32_swap_b32 v197, v199\n" \
"v_max_f32 v196, v196, v198\n" \
"v_max_f32 v197, v197, v199\n" \
"v_mov_b32 v198, v196\n" \
"v_mov_b32 v199, v197\n" \
"s_nop 0\n" \
"v_permlane16_swap_b32 v196, v198\n" \
"v_permlane16_swap_b32 v197, v199\n" \
"v_max_f32 v196, v196, v198\n" \
"v_max_f32 v197, v197, v199\n" \
"v_mul_f32 v56, s59, v196\n" \
"v_mul_f32 v57, s59, v197\n" \
"v_fma_f32 v28, v28, s59, -v56\n" \
"v_fma_f32 v29, v29, s59, -v56\n" \
"v_fma_f32 v30, v30, s59, -v56\n" \
"v_fma_f32 v31, v31, s59, -v56\n" \
"v_exp_f32 v28, v28\n" \
"v_exp_f32 v29, v29\n" \
"v_exp_f32 v30, v30\n" \
"v_exp_f32 v31, v31\n" \
"v_fma_f32 v32, v32, s59, -v57\n" \
"v_fma_f32 v33, v33, s59, -v57\n" \
"v_fma_f32 v34, v34, s59, -v57\n" \
"v_fma_f32 v35, v35, s59, -v57\n" \
"v_fma_f32 v36, v36, s59, -v57\n" \
"v_fma_f32 v37, v37, s59, -v57\n" \
"v_fma_f32 v38, v38, s59, -v57\n" \
"v_fma_f32 v39, v39, s59, -v57\n" \
"v_cvt_pk_bf16_f32 v40, v28, v29\n" \
"v_cvt_pk_bf16_f32 v41, v30, v31\n" \
"v_exp_f32 v32, v32\n" \
"v_exp_f32 v33, v33\n" \
"v_exp_f32 v34, v34\n" \
"v_mfma_f32_16x16x32_bf16 v[48:51], v[24:27], v[40:43], 0\n" \
"v_exp_f32 v35, v35\n" \
"v_mfma_f32_16x16x32_bf16 v[108:111], a[64:67], v[40:43], 0\n" \
"v_exp_f32 v36, v36\n" \
"v_mfma_f32_16x16x32_bf16 v[112:115], a[68:71], v[40:43], 0\n" \
"v_exp_f32 v37, v37\n" \
"v_mfma_f32_16x16x32_bf16 v[116:119], a[72:75], v[40:43], 0\n" \
"v_exp_f32 v38, v38\n" \
"v_mfma_f32_16x16x32_bf16 v[120:123], a[76:79], v[40:43], 0\n" \
"v_exp_f32 v39, v39\n" \
"v_mfma_f32_16x16x32_bf16 v[124:127], a[80:83], v[40:43], 0\n" \
"v_cvt_pk_bf16_f32 v44, v32, v33\n" \
"v_cvt_pk_bf16_f32 v45, v34, v35\n" \
"v_mfma_f32_16x16x32_bf16 v[128:131], a[84:87], v[40:43], 0\n" \
"v_cvt_pk_bf16_f32 v46, v36, v37\n" \
"v_cvt_pk_bf16_f32 v47, v38, v39\n" \
"v_mfma_f32_16x16x32_bf16 v[132:135], a[88:91], v[40:43], 0\n" \
"v_mfma_f32_16x16x32_bf16 v[136:139], a[92:95], v[40:43], 0\n" \
"v_mfma_f32_16x16x32_bf16 v[52:55], v[24:27], v[44:47], 0\n" \
"v_mfma_f32_16x16x32_bf16 v[140:143], a[64:67], v[44:47], 0\n" \
"v_mfma_f32_16x16x32_bf16 v[144:147], a[68:71], v[44:47], 0\n" \
"v_mfma_f32_16x16x32_bf16 v[148:151], a[72:75], v[44:47], 0\n" \
"v_mfma_f32_16x16x32_bf16 v[152:155], a[76:79], v[44:47], 0\n" \
"v_mfma_f32_16x16x32_bf16 v[156:159], a[80:83], v[44:47], 0\n" \
"v_mfma_f32_16x16x32_bf16 v[160:163], a[84:87], v[44:47], 0\n" \
"v_mfma_f32_16x16x32_bf16 v[164:167], a[88:91], v[44:47], 0\n" \
"v_mfma_f32_16x16x32_bf16 v[168:171], a[92:95], v[44:47], 0\n" \
"s_cmp_ge_u32 s56, 62\n" \
"s_cbranch_scc1 Lt0_w0%=\n" \
"s_waitcnt vmcnt(24)\n" \
"Lt0_w1%=:\n" \
"s_mov_b64 exec, 1\n" \
"global_store_dword v15, v13, s[54:55] sc1\n" \
"s_mov_b64 exec, -1\n" \
"s_add_u32 s61, s56, 1\n" \
"s_min_u32 s61, s61, 63\n" \
"v_cmp_gt_u32 vcc, s61, v7\n" \
"s_cbranch_vccnz Lt0_pr%=\n" \
"Lt0_prok%=:\n" \
"global_load_dwordx4 v[76:79], v2, s[46:47] offset:0 sc1\n" \
"global_load_dwordx4 v[80:83], v2, s[46:47] offset:512 sc1\n" \
"global_load_dwordx4 v[84:87], v2, s[46:47] offset:1024 sc1\n" \
"global_load_dwordx4 v[88:91], v2, s[46:47] offset:1536 sc1\n" \
"global_load_dwordx2 v[92:93], v3, s[48:49] offset:0 sc1\n" \
"global_load_dwordx2 v[94:95], v3, s[48:49] offset:256 sc1\n" \
"global_load_dwordx2 v[96:97], v3, s[48:49] offset:512 sc1\n" \
"global_load_dwordx2 v[98:99], v3, s[48:49] offset:768 sc1\n" \
"global_load_dwordx2 v[100:101], v3, s[48:49] offset:1024 sc1\n" \
"global_load_dwordx2 v[102:103], v3, s[48:49] offset:1280 sc1\n" \
"global_load_dwordx2 v[104:105], v3, s[48:49] offset:1536 sc1\n" \
"global_load_dwordx2 v[106:107], v3, s[48:49] offset:1792 sc1\n" \
"s_add_u32 s46, s46, 0x20000\n" \
"s_addc_u32 s47, s47, 0\n" \
"s_add_u32 s48, s48, 0x20000\n" \
"s_addc_u32 s49, s49, 0\n" \
"s_waitcnt vmcnt(11)\n" \
"v_mfma_f32_16x16x32_bf16 v[64:67], v[76:79], a[0:3], v[20:23]\n" \
"v_mfma_f32_16x16x32_bf16 v[68:71], v[76:79], a[16:19], v[20:23]\n" \
"s_waitcnt vmcnt(10)\n" \
"v_mfma_f32_16x16x32_bf16 v[64:67], v[80:83], a[4:7], v[64:67]\n" \
"v_mfma_f32_16x16x32_bf16 v[68:71], v[80:83], a[20:23], v[68:71]\n" \
"s_waitcnt vmcnt(9)\n" \
"v_mfma_f32_16x16x32_bf16 v[64:67], v[84:87], a[8:11], v[64:67]\n" \
"v_mfma_f32_16x16x32_bf16 v[68:71], v[84:87], a[24:27], v[68:71]\n" \
"s_waitcnt vmcnt(8)\n" \
"v_mfma_f32_16x16x32_bf16 v[64:67], v[88:91], a[12:15], v[64:67]\n" \
"v_mfma_f32_16x16x32_bf16 v[68:71], v[88:91], a[28:31], v[68:71]\n" \
"s_nop 3\n" \
"v_max3_f32 v196, v64, v65, v66\n" \
"v_max_f32 v196, v196, v67\n" \
"s_nop 1\n" \
"v_max3_f32 v197, v68, v69, v70\n" \
"v_max_f32 v197, v197, v71\n" \
"v_mov_b32 v198, v196\n" \
"v_mov_b32 v199, v197\n" \
"s_nop 0\n" \
"v_permlane32_swap_b32 v196, v198\n" \
"v_permlane32_swap_b32 v197, v199\n" \
"v_max_f32 v196, v196, v198\n" \
"v_max_f32 v197, v197, v199\n" \
"v_mov_b32 v198, v196\n" \
"v_mov_b32 v199, v197\n" \
"s_nop 0\n" \
"v_permlane16_swap_b32 v196, v198\n" \
"v_permlane16_swap_b32 v197, v199\n" \
"v_max_f32 v196, v196, v198\n" \
"v_max_f32 v197, v197, v199\n" \
"v_mul_f32 v196, s59, v196\n" \
"v_mul_f32 v197, s59, v197\n" \
"v_max_f32 v196, v196, v56\n" \
"v_max_f32 v197, v197, v57\n" \
"v_fma_f32 v64, v64, s59, -v196\n" \
"v_fma_f32 v65, v65, s59, -v196\n" \
"v_fma_f32 v66, v66, s59, -v196\n" \
"v_fma_f32 v67, v67, s59, -v196\n" \
"v_fma_f32 v68, v68, s59, -v197\n" \
"v_fma_f32 v69, v69, s59, -v197\n" \
"v_fma_f32 v70, v70, s59, -v197\n" \
"v_fma_f32 v71, v71, s59, -v197\n" \
"v_sub_f32 v194, v56, v196\n" \
"v_sub_f32 v195, v57, v197\n" \
"v_exp_f32 v64, v64\n" \
"v_exp_f32 v65, v65\n" \
"v_exp_f32 v66, v66\n" \
"v_exp_f32 v67, v67\n" \
"v_exp_f32 v68, v68\n" \
"v_exp_f32 v69, v69\n" \
"v_exp_f32 v70, v70\n" \
"v_exp_f32 v71, v71\n" \
"v_exp_f32 v194, v194\n" \
"v_exp_f32 v195, v195\n" \
"v_add_f32 v192, v64, v65\n" \
"v_add_f32 v196, v66, v67\n" \
"v_add_f32 v193, v68, v69\n" \
"v_add_f32 v197, v70, v71\n" \
"v_add_f32 v192, v192, v196\n" \
"v_add_f32 v193, v193, v197\n" \
"v_mov_b32 v198, v192\n" \
"v_mov_b32 v199, v193\n" \
"s_nop 0\n" \
"v_permlane32_swap_b32 v192, v198\n" \
"v_permlane32_swap_b32 v193, v199\n" \
"v_add_f32 v192, v192, v198\n" \
"v_add_f32 v193, v193, v199\n" \
"v_mov_b32 v198, v192\n" \
"v_mov_b32 v199, v193\n" \
"s_nop 0\n" \
"v_permlane16_swap_b32 v192, v198\n" \
"v_permlane16_swap_b32 v193, v199\n" \
"v_add_f32 v192, v192, v198\n" \
"v_add_f32 v193, v193, v199\n" \
"v_fma_f32 v192, v194, v48, v192\n" \
"v_fma_f32 v193, v195, v52, v193\n" \
"v_rcp_f32 v192, v192\n" \
"v_rcp_f32 v193, v193\n" \
"s_nop 0\n" \
"v_mul_f32 v188, v194, v192\n" \
"v_mul_f32 v190, v195, v193\n" \
"v_mul_f32 v64, v64, v192\n" \
"v_mul_f32 v65, v65, v192\n" \
"v_mul_f32 v66, v66, v192\n" \
"v_mul_f32 v67, v67, v192\n" \
"v_mul_f32 v68, v68, v193\n" \
"v_mul_f32 v69, v69, v193\n" \
"v_mul_f32 v70, v70, v193\n" \
"v_mul_f32 v71, v71, v193\n" \
"v_pk_mul_f32 v[108:109], v[108:109], v[188:189] op_sel_hi:[1,0]\n" \
"v_pk_mul_f32 v[110:111], v[110:111], v[188:189] op_sel_hi:[1,0]\n" \
"v_cvt_pk_bf16_f32 v72, v64, v65\n" \
"v_cvt_pk_bf16_f32 v73, v66, v67\n" \
"v_cvt_pk_bf16_f32 v74, v68, v69\n" \
"v_cvt_pk_bf16_f32 v75, v70, v71\n" \
"v_pk_mul_f32 v[112:113], v[112:113], v[188:189] op_sel_hi:[1,0]\n" \
"v_pk_mul_f32 v[114:115], v[114:115], v[188:189] op_sel_hi:[1,0]\n" \
"s_waitcnt vmcnt(0)\n" \
"v_mfma_f32_16x16x16_bf16 v[108:111], v[92:93], v[72:73], v[108:111]\n" \
"v_pk_mul_f32 v[116:117], v[116:117], v[188:189] op_sel_hi:[1,0]\n" \
"v_pk_mul_f32 v[118:119], v[118:119], v[188:189] op_sel_hi:[1,0]\n" \
"v_mfma_f32_16x16x16_bf16 v[112:115], v[94:95], v[72:73], v[112:115]\n" \
"v_pk_mul_f32 v[120:121], v[120:121], v[188:189] op_sel_hi:[1,0]\n" \
"v_pk_mul_f32 v[122:123], v[122:123], v[188:189] op_sel_hi:[1,0]\n" \
"v_mfma_f32_16x16x16_bf16 v[116:119], v[96:97], v[72:73], v[116:119]\n" \
"v_pk_mul_f32 v[124:125], v[124:125], v[188:189] op_sel_hi:[1,0]\n" \
"v_pk_mul_f32 v[126:127], v[126:127], v[188:189] op_sel_hi:[1,0]\n" \
"v_mfma_f32_16x16x16_bf16 v[120:123], v[98:99], v[72:73], v[120:123]\n" \
"v_pk_mul_f32 v[128:129], v[128:129], v[188:189] op_sel_hi:[1,0]\n" \
"v_pk_mul_f32 v[130:131], v[130:131], v[188:189] op_sel_hi:[1,0]\n" \
"v_cvt_pk_bf16_f32 v172, v108, v109\n" \
"v_cvt_pk_bf16_f32 v173, v110, v111\n" \
"v_mfma_f32_16x16x16_bf16 v[124:127], v[100:101], v[72:73], v[124:127]\n" \
"v_pk_mul_f32 v[132:133], v[132:133], v[188:189] op_sel_hi:[1,0]\n" \
"v_pk_mul_f32 v[134:135], v[134:135], v[188:189] op_sel_hi:[1,0]\n" \
"v_cvt_pk_bf16_f32 v174, v112, v113\n" \
"v_cvt_pk_bf16_f32 v175, v114, v115\n" \
"v_min_u32 v172, -2, v172\n" \
"v_min_u32 v173, -2, v173\n" \
"v_min_u32 v174, -2, v174\n" \
"v_min_u32 v175, -2, v175\n" \
"global_store_dwordx4 v0, v[172:175], s[50:51] offset:0 sc1\n" \
"v_mfma_f32_16x16x16_bf16 v[128:131], v[102:103], v[72:73], v[128:131]\n" \
"v_pk_mul_f32 v[136:137], v[136:137], v[188:189] op_sel_hi:[1,0]\n" \
"v_pk_mul_f32 v[138:139], v[138:139], v[188:189] op_sel_hi:[1,0]\n" \
"v_cvt_pk_bf16_f32 v176, v116, v117\n" \
"v_cvt_pk_bf16_f32 v177, v118, v119\n" \
"v_mfma_f32_16x16x16_bf16 v[132:135], v[104:105], v[72:73], v[132:135]\n" \
"v_pk_mul_f32 v[140:141], v[140:141], v[190:191] op_sel_hi:[1,0]\n" \
"v_pk_mul_f32 v[142:143], v[142:143], v[190:191] op_sel_hi:[1,0]\n" \
"v_cvt_pk_bf16_f32 v178, v120, v121\n" \
"v_cvt_pk_bf16_f32 v179, v122, v123\n" \
"v_min_u32 v176, -2, v176\n" \
"v_min_u32 v177, -2, v177\n" \
"v_min_u32 v178, -2, v178\n" \
"v_min_u32 v179, -2, v179\n" \
"global_store_dwordx4 v0, v[176:179], s[50:51] offset:1024 sc1\n" \
"v_mfma_f32_16x16x16_bf16 v[136:139], v[106:107], v[72:73], v[136:139]\n" \
"v_pk_mul_f32 v[144:145], v[144:145], v[190:191] op_sel_hi:[1,0]\n" \
"v_pk_mul_f32 v[146:147], v[146:147], v[190:191] op_sel_hi:[1,0]\n" \
"v_cvt_pk_bf16_f32 v180, v124, v125\n" \
"v_cvt_pk_bf16_f32 v181, v126, v127\n" \
"v_mfma_f32_16x16x16_bf16 v[140:143], v[92:93], v[74:75], v[140:143]\n" \
"v_pk_mul_f32 v[148:149], v[148:149], v[190:191] op_sel_hi:[1,0]\n" \
"v_pk_mul_f32 v[150:151], v[150:151], v[190:191] op_sel_hi:[1,0]\n" \
"v_cvt_pk_bf16_f32 v182, v128, v129\n" \
"v_cvt_pk_bf16_f32 v183, v130, v131\n" \
"v_min_u32 v180, -2, v180\n" \
"v_min_u32 v181, -2, v181\n" \
"v_min_u32 v182, -2, v182\n" \
"v_min_u32 v183, -2, v183\n" \
"global_store_dwordx4 v0, v[180:183], s[50:51] offset:2048 sc1\n" \
"v_mfma_f32_16x16x16_bf16 v[144:147], v[94:95], v[74:75], v[144:147]\n" \
"v_pk_mul_f32 v[152:153], v[152:153], v[190:191] op_sel_hi:[1,0]\n" \
"v_pk_mul_f32 v[154:155], v[154:155], v[190:191] op_sel_hi:[1,0]\n" \
"v_cvt_pk_bf16_f32 v184, v132, v133\n" \
"v_cvt_pk_bf16_f32 v185, v134, v135\n" \
"v_mfma_f32_16x16x16_bf16 v[148:151], v[96:97], v[74:75], v[148:151]\n" \
"v_pk_mul_f32 v[156:157], v[156:157], v[190:191] op_sel_hi:[1,0]\n" \
"v_pk_mul_f32 v[158:159], v[158:159], v[190:191] op_sel_hi:[1,0]\n" \
"v_cvt_pk_bf16_f32 v186, v136, v137\n" \
"v_cvt_pk_bf16_f32 v187, v138, v139\n" \
"v_min_u32 v184, -2, v184\n" \
"v_min_u32 v185, -2, v185\n" \
"v_min_u32 v186, -2, v186\n" \
"v_min_u32 v187, -2, v187\n" \
"global_store_dwordx4 v0, v[184:187], s[50:51] offset:3072 sc1\n" \
"v_mfma_f32_16x16x16_bf16 v[152:155], v[98:99], v[74:75], v[152:155]\n" \
"v_pk_mul_f32 v[160:161], v[160:161], v[190:191] op_sel_hi:[1,0]\n" \
"v_pk_mul_f32 v[162:163], v[162:163], v[190:191] op_sel_hi:[1,0]\n" \
"v_cvt_pk_bf16_f32 v172, v140, v141\n" \
"v_cvt_pk_bf16_f32 v173, v142, v143\n" \
"v_mfma_f32_16x16x16_bf16 v[156:159], v[100:101], v[74:75], v[156:159]\n" \
"v_pk_mul_f32 v[164:165], v[164:165], v[190:191] op_sel_hi:[1,0]\n" \
"v_pk_mul_f32 v[166:167], v[166:167], v[190:191] op_sel_hi:[1,0]\n" \
"v_cvt_pk_bf16_f32 v174, v144, v145\n" \
"v_cvt_pk_bf16_f32 v175, v146, v147\n" \
"v_min_u32 v172, -2, v172\n" \
"v_min_u32 v173, -2, v173\n" \
"v_min_u32 v174, -2, v174\n" \
"v_min_u32 v175, -2, v175\n" \
"global_store_dwordx4 v0, v[172:175], s[52:53] offset:0 sc1\n" \
"v_mfma_f32_16x16x16_bf16 v[160:163], v[102:103], v[74:75], v[160:163]\n" \
"v_pk_mul_f32 v[168:169], v[168:169], v[190:191] op_sel_hi:[1,0]\n" \
"v_pk_mul_f32 v[170:171], v[170:171], v[190:191] op_sel_hi:[1,0]\n" \
"v_cvt_pk_bf16_f32 v176, v148, v149\n" \
"v_cvt_pk_bf16_f32 v177, v150, v151\n" \
"v_mfma_f32_16x16x16_bf16 v[164:167], v[104:105], v[74:75], v[164:167]\n" \
"v_cvt_pk_bf16_f32 v178, v152, v153\n" \
"v_cvt_pk_bf16_f32 v179, v154, v155\n" \
"v_min_u32 v176, -2, v176\n" \
"v_min_u32 v177, -2, v177\n" \
"v_min_u32 v178, -2, v178\n" \
"v_min_u32 v179, -2, v179\n" \
"global_store_dwordx4 v0, v[176:179], s[52:53] offset:1024 sc1\n" \
"v_mfma_f32_16x16x16_bf16 v[168:171], v[106:107], v[74:75], v[168:171]\n" \
"v_cvt_pk_bf16_f32 v180, v156, v157\n" \
"v_cvt_pk_bf16_f32 v181, v158, v159\n" \
"v_cvt_pk_bf16_f32 v182, v160, v161\n" \
"v_cvt_pk_bf16_f32 v183, v162, v163\n" \
"v_min_u32 v180, -2, v180\n" \
"v_min_u32 v181, -2, v181\n" \
"v_min_u32 v182, -2, v182\n" \
"v_min_u32 v183, -2, v183\n" \
"global_store_dwordx4 v0, v[180:183], s[52:53] offset:2048 sc1\n" \
"s_nop 3\n" \
"v_cvt_pk_bf16_f32 v184, v164, v165\n" \
"v_cvt_pk_bf16_f32 v185, v166, v167\n" \
"s_nop 3\n" \
"v_cvt_pk_bf16_f32 v186, v168, v169\n" \
"v_cvt_pk_bf16_f32 v187, v170, v171\n" \
"v_min_u32 v184, -2, v184\n" \
"v_min_u32 v185, -2, v185\n" \
"v_min_u32 v186, -2, v186\n" \
"v_min_u32 v187, -2, v187\n" \
"global_store_dwordx4 v0, v[184:187], s[52:53] offset:3072 sc1\n" \
"s_nop 3\n" \
"s_add_u32 s61, s56, 1\n" \
"v_mov_b32 v13, s61\n" \
"s_add_u32 s50, s50, 0x80000\n" \
"s_addc_u32 s51, s51, 0\n" \
"s_add_u32 s52, s52, 0x80000\n" \
"s_addc_u32 s53, s53, 0\n" \
"s_add_u32 s56, s56, 2\n" \
"s_cmp_ge_u32 s56, 64\n" \
"s_cbranch_scc1 Lt_end%=\n" \
"Lt1_top%=:\n" \
"global_load_dword v7, v5, s[54:55] sc1\n" \
"s_add_u32 s61, s56, 2\n" \
"s_cmp_ge_u32 s61, 64\n" \
"s_cbranch_scc1 Lt1_nopf%=\n" \
"s_cmp_gt_u32 s57, s61\n" \
"s_cbranch_scc0 Lt1_t1%=\n" \
"Lt1_t1ok%=:\n" \
"global_load_dwordx4 a[32:35], v0, s[42:43] offset:0 sc1\n" \
"global_load_dwordx4 a[0:3], v0, s[40:41] offset:0 sc1\n" \
"global_load_dwordx4 a[36:39], v0, s[42:43] offset:1024 sc1\n" \
"global_load_dwordx4 a[4:7], v0, s[40:41] offset:1024 sc1\n" \
"global_load_dwordx4 a[40:43], v0, s[42:43] offset:2048 sc1\n" \
"global_load_dwordx4 a[8:11], v0, s[40:41] offset:2048 sc1\n" \
"global_load_dwordx4 a[44:47], v0, s[42:43] offset:3072 sc1\n" \
"global_load_dwordx4 a[12:15], v0, s[40:41] offset:3072 sc1\n" \
"global_load_dwordx4 a[48:51], v1, s[42:43] offset:0 sc1\n" \
"global_load_dwordx4 a[16:19], v1, s[40:41] offset:0 sc1\n" \
"global_load_dwordx4 a[52:55], v1, s[42:43] offset:1024 sc1\n" \
"global_load_dwordx4 a[20:23], v1, s[40:41] offset:1024 sc1\n" \
"global_load_dwordx4 a[56:59], v1, s[42:43] offset:2048 sc1\n" \
"global_load_dwordx4 a[24:27], v1, s[40:41] offset:2048 sc1\n" \
"global_load_dwordx4 a[60:63], v1, s[42:43] offset:3072 sc1\n" \
"global_load_dwordx4 a[28:31], v1, s[40:41] offset:3072 sc1\n" \
"global_load_dwordx4 a[64:67], v0, s[44:45] offset:0 sc1\n" \
"global_load_dwordx4 a[68:71], v0, s[44:45] offset:1024 sc1\n" \
"global_load_dwordx4 a[72:75], v0, s[44:45] offset:2048 sc1\n" \
"global_load_dwordx4 a[76:79], v0, s[44:45] offset:3072 sc1\n" \
"global_load_dwordx4 a[80:83], v1, s[44:45] offset:0 sc1\n" \
"global_load_dwordx4 a[84:87], v1, s[44:45] offset:1024 sc1\n" \
"global_load_dwordx4 a[88:91], v1, s[44:45] offset:2048 sc1\n" \
"global_load_dwordx4 a[92:95], v1, s[44:45] offset:3072 sc1\n" \
"s_add_u32 s40, s40, 0x80000\n" \
"s_addc_u32 s41, s41, 0\n" \
"s_add_u32 s42, s42, 0x80000\n" \
"s_addc_u32 s43, s43, 0\n" \
"s_add_u32 s44, s44, 0x80000\n" \
"s_addc_u32 s45, s45, 0\n" \
"Lt1_nopf%=:\n" \
"v_mfma_f32_16x16x32_bf16 v[28:31], a[128:131], a[96:99], v[16:19]\n" \
"v_mfma_f32_16x16x32_bf16 v[32:35], a[128:131], a[112:115], 0\n" \
"v_mfma_f32_16x16x32_bf16 v[36:39], a[144:147], a[112:115], v[16:19]\n" \
"v_mfma_f32_16x16x32_bf16 v[28:31], a[132:135], a[100:103], v[28:31]\n" \
"v_mfma_f32_16x16x32_bf16 v[32:35], a[132:135], a[116:119], v[32:35]\n" \
"v_mfma_f32_16x16x32_bf16 v[36:39], a[148:151], a[116:119], v[36:39]\n" \
"v_mfma_f32_16x16x32_bf16 v[28:31], a[136:139], a[104:107], v[28:31]\n" \
"v_mfma_f32_16x16x32_bf16 v[32:35], a[136:139], a[120:123], v[32:35]\n" \
"v_mfma_f32_16x16x32_bf16 v[36:39], a[152:155], a[120:123], v[36:39]\n" \
"v_mfma_f32_16x16x32_bf16 v[28:31], a[140:143], a[108:111], v[28:31]\n" \
"v_mfma_f32_16x16x32_bf16 v[32:35], a[140:143], a[124:127], v[32:35]\n" \
"v_mfma_f32_16x16x32_bf16 v[36:39], a[156:159], a[124:127], v[36:39]\n" \
"s_nop 0\n" \
"v_max3_f32 v196, v28, v29, v30\n" \
"v_max_f32 v196, v196, v31\n" \
"s_nop 1\n" \
"v_max3_f32 v197, v32, v33, v34\n" \
"s_nop 3\n" \
"v_max3_f32 v197, v197, v35, v36\n" \
"v_max3_f32 v197, v197, v37, v38\n" \
"v_max_f32 v197, v197, v39\n" \
"v_mov_b32 v198, v196\n" \
"v_mov_b32 v199, v197\n" \
"s_nop 0\n" \
"v_permlane32_swap_b32 v196, v198\n" \
"v_permlane32_swap_b32 v197, v199\n" \
"v_max_f32 v196, v196, v198\n" \
"v_max_f32 v197, v197, v199\n" \
"v_mov_b32 v198, v196\n" \
"v_mov_b32 v199, v197\n" \
"s_nop 0\n" \
"v_permlane16_swap_b32 v196, v198\n" \
"v_permlane16_swap_b32 v197, v199\n" \
"v_max_f32 v196, v196, v198\n" \
"v_max_f32 v197, v197, v199\n" \
"v_mul_f32 v56, s59, v196\n" \
"v_mul_f32 v57, s59, v197\n" \
"v_fma_f32 v28, v28, s59, -v56\n" \
"v_fma_f32 v29, v29, s59, -v56\n" \
"v_fma_f32 v30, v30, s59, -v56\n" \
"v_fma_f32 v31, v31, s59, -v56\n" \
"v_exp_f32 v28, v28\n" \
"v_exp_f32 v29, v29\n" \
"v_exp_f32 v30, v30\n" \
"v_exp_f32 v31, v31\n" \
"v_fma_f32 v32, v32, s59, -v57\n" \
"v_fma_f32 v33, v33, s59, -v57\n" \
"v_fma_f32 v34, v34, s59, -v57\n" \
"v_fma_f32 v35, v35, s59, -v57\n" \
"v_fma_f32 v36, v36, s59, -v57\n" \
"v_fma_f32 v37, v37, s59, -v57\n" \
"v_fma_f32 v38, v38, s59, -v57\n" \
"v_fma_f32 v39, v39, s59, -v57\n" \
"v_cvt_pk_bf16_f32 v40, v28, v29\n" \
"v_cvt_pk_bf16_f32 v41, v30, v31\n" \
"v_exp_f32 v32, v32\n" \
"v_exp_f32 v33, v33\n" \
"v_exp_f32 v34, v34\n" \
"v_mfma_f32_16x16x32_bf16 v[48:51], v[24:27], v[40:43], 0\n" \
"v_exp_f32 v35, v35\n" \
"v_mfma_f32_16x16x32_bf16 v[108:111], a[160:163], v[40:43], 0\n" \
"v_exp_f32 v36, v36\n" \
"v_mfma_f32_16x16x32_bf16 v[112:115], a[164:167], v[40:43], 0\n" \
"v_exp_f32 v37, v37\n" \
"v_mfma_f32_16x16x32_bf16 v[116:119], a[168:171], v[40:43], 0\n" \
"v_exp_f32 v38, v38\n" \
"v_mfma_f32_16x16x32_bf16 v[120:123], a[172:175], v[40:43], 0\n" \
"v_exp_f32 v39, v39\n" \
"v_mfma_f32_16x16x32_bf16 v[124:127], a[176:179], v[40:43], 0\n" \
"v_cvt_pk_bf16_f32 v44, v32, v33\n" \
"v_cvt_pk_bf16_f32 v45, v34, v35\n" \
"v_mfma_f32_16x16x32_bf16 v[128:131], a[180:183], v[40:43], 0\n" \
"v_cvt_pk_bf16_f32 v46, v36, v37\n" \
"v_cvt_pk_bf16_f32 v47, v38, v39\n" \
"v_mfma_f32_16x16x32_bf16 v[132:135], a[184:187], v[40:43], 0\n" \
"v_mfma_f32_16x16x32_bf16 v[136:139], a[188:191], v[40:43], 0\n" \
"v_mfma_f32_16x16x32_bf16 v[52:55], v[24:27], v[44:47], 0\n" \
"v_mfma_f32_16x16x32_bf16 v[140:143], a[160:163], v[44:47], 0\n" \
"v_mfma_f32_16x16x32_bf16 v[144:147], a[164:167], v[44:47], 0\n" \
"v_mfma_f32_16x16x32_bf16 v[148:151], a[168:171], v[44:47], 0\n" \
"v_mfma_f32_16x16x32_bf16 v[152:155], a[172:175], v[44:47], 0\n" \
"v_mfma_f32_16x16x32_bf16 v[156:159], a[176:179], v[44:47], 0\n" \
"v_mfma_f32_16x16x32_bf16 v[160:163], a[180:183], v[44:47], 0\n" \
"v_mfma_f32_16x16x32_bf16 v[164:167], a[184:187], v[44:47], 0\n" \
"v_mfma_f32_16x16x32_bf16 v[168:171], a[188:191], v[44:47], 0\n" \
"s_cmp_ge_u32 s56, 62\n" \
"s_cbranch_scc1 Lt1_w0%=\n" \
"s_waitcnt vmcnt(24)\n" \
"Lt1_w1%=:\n" \
"s_mov_b64 exec, 1\n" \
"global_store_dword v15, v13, s[54:55] sc1\n" \
"s_mov_b64 exec, -1\n" \
"s_add_u32 s61, s56, 1\n" \
"s_min_u32 s61, s61, 63\n" \
"v_cmp_gt_u32 vcc, s61, v7\n" \
"s_cbranch_vccnz Lt1_pr%=\n" \
"Lt1_prok%=:\n" \
"s_cmp_eq_u32 s56, 63\n" \
"s_cbranch_scc1 Lt_last%=\n" \
"global_load_dwordx4 v[76:79], v2, s[46:47] offset:0 sc1\n" \
"global_load_dwordx4 v[80:83], v2, s[46:47] offset:512 sc1\n" \
"global_load_dwordx4 v[84:87], v2, s[46:47] offset:1024 sc1\n" \
"global_load_dwordx4 v[88:91], v2, s[46:47] offset:1536 sc1\n" \
"global_load_dwordx2 v[92:93], v3, s[48:49] offset:0 sc1\n" \
"global_load_dwordx2 v[94:95], v3, s[48:49] offset:256 sc1\n" \
"global_load_dwordx2 v[96:97], v3, s[48:49] offset:512 sc1\n" \
"global_load_dwordx2 v[98:99], v3, s[48:49] offset:768 sc1\n" \
"global_load_dwordx2 v[100:101], v3, s[48:49] offset:1024 sc1\n" \
"global_load_dwordx2 v[102:103], v3, s[48:49] offset:1280 sc1\n" \
"global_load_dwordx2 v[104:105], v3, s[48:49] offset:1536 sc1\n" \
"global_load_dwordx2 v[106:107], v3, s[48:49] offset:1792 sc1\n" \
"Lt_lastok%=:\n" \
"s_add_u32 s46, s46, 0x20000\n" \
"s_addc_u32 s47, s47, 0\n" \
"s_add_u32 s48, s48, 0x20000\n" \
"s_addc_u32 s49, s49, 0\n" \
"s_waitcnt vmcnt(11)\n" \
"v_mfma_f32_16x16x32_bf16 v[64:67], v[76:79], a[96:99], v[20:23]\n" \
"v_mfma_f32_16x16x32_bf16 v[68:71], v[76:79], a[112:115], v[20:23]\n" \
"s_waitcnt vmcnt(10)\n" \
"v_mfma_f32_16x16x32_bf16 v[64:67], v[80:83], a[100:103], v[64:67]\n" \
"v_mfma_f32_16x16x32_bf16 v[68:71], v[80:83], a[116:119], v[68:71]\n" \
"s_waitcnt vmcnt(9)\n" \
"v_mfma_f32_16x16x32_bf16 v[64:67], v[84:87], a[104:107], v[64:67]\n" \
"v_mfma_f32_16x16x32_bf16 v[68:71], v[84:87], a[120:123], v[68:71]\n" \
"s_waitcnt vmcnt(8)\n" \
"v_mfma_f32_16x16x32_bf16 v[64:67], v[88:91], a[108:111], v[64:67]\n" \
"v_mfma_f32_16x16x32_bf16 v[68:71], v[88:91], a[124:127], v[68:71]\n" \
"s_nop 3\n" \
"v_max3_f32 v196, v64, v65, v66\n" \
"v_max_f32 v196, v196, v67\n" \
"s_nop 1\n" \
"v_max3_f32 v197, v68, v69, v70\n" \
"v_max_f32 v197, v197, v71\n" \
"v_mov_b32 v198, v196\n" \
"v_mov_b32 v199, v197\n" \
"s_nop 0\n" \
"v_permlane32_swap_b32 v196, v198\n" \
"v_permlane32_swap_b32 v197, v199\n" \
"v_max_f32 v196, v196, v198\n" \
"v_max_f32 v197, v197, v199\n" \
"v_mov_b32 v198, v196\n" \
"v_mov_b32 v199, v197\n" \
"s_nop 0\n" \
"v_permlane16_swap_b32 v196, v198\n" \
"v_permlane16_swap_b32 v197, v199\n" \
"v_max_f32 v196, v196, v198\n" \
"v_max_f32 v197, v197, v199\n" \
"v_mul_f32 v196, s59, v196\n" \
"v_mul_f32 v197, s59, v197\n" \
"v_max_f32 v196, v196, v56\n" \
"v_max_f32 v197, v197, v57\n" \
"v_fma_f32 v64, v64, s59, -v196\n" \
"v_fma_f32 v65, v65, s59, -v196\n" \
"v_fma_f32 v66, v66, s59, -v196\n" \
"v_fma_f32 v67, v67, s59, -v196\n" \
"v_fma_f32 v68, v68, s59, -v197\n" \
"v_fma_f32 v69, v69, s59, -v197\n" \
"v_fma_f32 v70, v70, s59, -v197\n" \
"v_fma_f32 v71, v71, s59, -v197\n" \
"v_sub_f32 v194, v56, v196\n" \
"v_sub_f32 v195, v57, v197\n" \
"v_exp_f32 v64, v64\n" \
"v_exp_f32 v65, v65\n" \
"v_exp_f32 v66, v66\n" \
"v_exp_f32 v67, v67\n" \
"v_exp_f32 v68, v68\n" \
"v_exp_f32 v69, v69\n" \
"v_exp_f32 v70, v70\n" \
"v_exp_f32 v71, v71\n" \
"v_exp_f32 v194, v194\n" \
"v_exp_f32 v195, v195\n" \
"v_add_f32 v192, v64, v65\n" \
"v_add_f32 v196, v66, v67\n" \
"v_add_f32 v193, v68, v69\n" \
"v_add_f32 v197, v70, v71\n" \
"v_add_f32 v192, v192, v196\n" \
"v_add_f32 v193, v193, v197\n" \
"v_mov_b32 v198, v192\n" \
"v_mov_b32 v199, v193\n" \
"s_nop 0\n" \
"v_permlane32_swap_b32 v192, v198\n" \
"v_permlane32_swap_b32 v193, v199\n" \
"v_add_f32 v192, v192, v198\n" \
"v_add_f32 v193, v193, v199\n" \
"v_mov_b32 v198, v192\n" \
"v_mov_b32 v199, v193\n" \
"s_nop 0\n" \
"v_permlane16_swap_b32 v192, v198\n" \
"v_permlane16_swap_b32 v193, v199\n" \
"v_add_f32 v192, v192, v198\n" \
"v_add_f32 v193, v193, v199\n" \
"v_fma_f32 v192, v194, v48, v192\n" \
"v_fma_f32 v193, v195, v52, v193\n" \
"v_rcp_f32 v192, v192\n" \
"v_rcp_f32 v193, v193\n" \
"s_nop 0\n" \
"v_mul_f32 v188, v194, v192\n" \
"v_mul_f32 v190, v195, v193\n" \
"v_mul_f32 v64, v64, v192\n" \
"v_mul_f32 v65, v65, v192\n" \
"v_mul_f32 v66, v66, v192\n" \
"v_mul_f32 v67, v67, v192\n" \
"v_mul_f32 v68, v68, v193\n" \
"v_mul_f32 v69, v69, v193\n" \
"v_mul_f32 v70, v70, v193\n" \
"v_mul_f32 v71, v71, v193\n" \
"v_pk_mul_f32 v[108:109], v[108:109], v[188:189] op_sel_hi:[1,0]\n" \
"v_pk_mul_f32 v[110:111], v[110:111], v[188:189] op_sel_hi:[1,0]\n" \
"v_cvt_pk_bf16_f32 v72, v64, v65\n" \
"v_cvt_pk_bf16_f32 v73, v66, v67\n" \
"v_cvt_pk_bf16_f32 v74, v68, v69\n" \
"v_cvt_pk_bf16_f32 v75, v70, v71\n" \
"v_pk_mul_f32 v[112:113], v[112:113], v[188:189] op_sel_hi:[1,0]\n" \
"v_pk_mul_f32 v[114:115], v[114:115], v[188:189] op_sel_hi:[1,0]\n" \
"s_waitcnt vmcnt(0)\n" \
"v_mfma_f32_16x16x16_bf16 v[108:111], v[92:93], v[72:73], v[108:111]\n" \
"v_pk_mul_f32 v[116:117], v[116:117], v[188:189] op_sel_hi:[1,0]\n" \
"v_pk_mul_f32 v[118:119], v[118:119], v[188:189] op_sel_hi:[1,0]\n" \
"v_mfma_f32_16x16x16_bf16 v[112:115], v[94:95], v[72:73], v[112:115]\n" \
"v_pk_mul_f32 v[120:121], v[120:121], v[188:189] op_sel_hi:[1,0]\n" \
"v_pk_mul_f32 v[122:123], v[122:123], v[188:189] op_sel_hi:[1,0]\n" \
"v_mfma_f32_16x16x16_bf16 v[116:119], v[96:97], v[72:73], v[116:119]\n" \
"v_pk_mul_f32 v[124:125], v[124:125], v[188:189] op_sel_hi:[1,0]\n" \
"v_pk_mul_f32 v[126:127], v[126:127], v[188:189] op_sel_hi:[1,0]\n" \
"v_mfma_f32_16x16x16_bf16 v[120:123], v[98:99], v[72:73], v[120:123]\n" \
"v_pk_mul_f32 v[128:129], v[128:129], v[188:189] op_sel_hi:[1,0]\n" \
"v_pk_mul_f32 v[130:131], v[130:131], v[188:189] op_sel_hi:[1,0]\n" \
"v_cvt_pk_bf16_f32 v172, v108, v109\n" \
"v_cvt_pk_bf16_f32 v173, v110, v111\n" \
"v_mfma_f32_16x16x16_bf16 v[124:127], v[100:101], v[72:73], v[124:127]\n" \
"v_pk_mul_f32 v[132:133], v[132:133], v[188:189] op_sel_hi:[1,0]\n" \
"v_pk_mul_f32 v[134:135], v[134:135], v[188:189] op_sel_hi:[1,0]\n" \
"v_cvt_pk_bf16_f32 v174, v112, v113\n" \
"v_cvt_pk_bf16_f32 v175, v114, v115\n" \
"v_min_u32 v172, -2, v172\n" \
"v_min_u32 v173, -2, v173\n" \
"v_min_u32 v174, -2, v174\n" \
"v_min_u32 v175, -2, v175\n" \
"global_store_dwordx4 v0, v[172:175], s[50:51] offset:0 sc1\n" \
"v_mfma_f32_16x16x16_bf16 v[128:131], v[102:103], v[72:73], v[128:131]\n" \
"v_pk_mul_f32 v[136:137], v[136:137], v[188:189] op_sel_hi:[1,0]\n" \
"v_pk_mul_f32 v[138:139], v[138:139], v[188:189] op_sel_hi:[1,0]\n" \
"v_cvt_pk_bf16_f32 v176, v116, v117\n" \
"v_cvt_pk_bf16_f32 v177, v118, v119\n" \
"v_mfma_f32_16x16x16_bf16 v[132:135], v[104:105], v[72:73], v[132:135]\n" \
"v_pk_mul_f32 v[140:141], v[140:141], v[190:191] op_sel_hi:[1,0]\n" \
"v_pk_mul_f32 v[142:143], v[142:143], v[190:191] op_sel_hi:[1,0]\n" \
"v_cvt_pk_bf16_f32 v178, v120, v121\n" \
"v_cvt_pk_bf16_f32 v179, v122, v123\n" \
"v_min_u32 v176, -2, v176\n" \
"v_min_u32 v177, -2, v177\n" \
"v_min_u32 v178, -2, v178\n" \
"v_min_u32 v179, -2, v179\n" \
"global_store_dwordx4 v0, v[176:179], s[50:51] offset:1024 sc1\n" \
"v_mfma_f32_16x16x16_bf16 v[136:139], v[106:107], v[72:73], v[136:139]\n" \
"v_pk_mul_f32 v[144:145], v[144:145], v[190:191] op_sel_hi:[1,0]\n" \
"v_pk_mul_f32 v[146:147], v[146:147], v[190:191] op_sel_hi:[1,0]\n" \
"v_cvt_pk_bf16_f32 v180, v124, v125\n" \
"v_cvt_pk_bf16_f32 v181, v126, v127\n" \
"v_mfma_f32_16x16x16_bf16 v[140:143], v[92:93], v[74:75], v[140:143]\n" \
"v_pk_mul_f32 v[148:149], v[148:149], v[190:191] op_sel_hi:[1,0]\n" \
"v_pk_mul_f32 v[150:151], v[150:151], v[190:191] op_sel_hi:[1,0]\n" \
"v_cvt_pk_bf16_f32 v182, v128, v129\n" \
"v_cvt_pk_bf16_f32 v183, v130, v131\n" \
"v_min_u32 v180, -2, v180\n" \
"v_min_u32 v181, -2, v181\n" \
"v_min_u32 v182, -2, v182\n" \
"v_min_u32 v183, -2, v183\n" \
"global_store_dwordx4 v0, v[180:183], s[50:51] offset:2048 sc1\n" \
"v_mfma_f32_16x16x16_bf16 v[144:147], v[94:95], v[74:75], v[144:147]\n" \
"v_pk_mul_f32 v[152:153], v[152:153], v[190:191] op_sel_hi:[1,0]\n" \
"v_pk_mul_f32 v[154:155], v[154:155], v[190:191] op_sel_hi:[1,0]\n" \
"v_cvt_pk_bf16_f32 v184, v132, v133\n" \
"v_cvt_pk_bf16_f32 v185, v134, v135\n" \
"v_mfma_f32_16x16x16_bf16 v[148:151], v[96:97], v[74:75], v[148:151]\n" \
"v_pk_mul_f32 v[156:157], v[156:157], v[190:191] op_sel_hi:[1,0]\n" \
"v_pk_mul_f32 v[158:159], v[158:159], v[190:191] op_sel_hi:[1,0]\n" \
"v_cvt_pk_bf16_f32 v186, v136, v137\n" \
"v_cvt_pk_bf16_f32 v187, v138, v139\n" \
"v_min_u32 v184, -2, v184\n" \
"v_min_u32 v185, -2, v185\n" \
"v_min_u32 v186, -2, v186\n" \
"v_min_u32 v187, -2, v187\n" \
"global_store_dwordx4 v0, v[184:187], s[50:51] offset:3072 sc1\n" \
"v_mfma_f32_16x16x16_bf16 v[152:155], v[98:99], v[74:75], v[152:155]\n" \
"v_pk_mul_f32 v[160:161], v[160:161], v[190:191] op_sel_hi:[1,0]\n" \
"v_pk_mul_f32 v[162:163], v[162:163], v[190:191] op_sel_hi:[1,0]\n" \
"v_cvt_pk_bf16_f32 v172, v140, v141\n" \
"v_cvt_pk_bf16_f32 v173, v142, v143\n" \
"v_mfma_f32_16x16x16_bf16 v[156:159], v[100:101], v[74:75], v[156:159]\n" \
"v_pk_mul_f32 v[164:165], v[164:165], v[190:191] op_sel_hi:[1,0]\n" \
"v_pk_mul_f32 v[166:167], v[166:167], v[190:191] op_sel_hi:[1,0]\n" \
"v_cvt_pk_bf16_f32 v174, v144, v145\n" \
"v_cvt_pk_bf16_f32 v175, v146, v147\n" \
"v_min_u32 v172, -2, v172\n" \
"v_min_u32 v173, -2, v173\n" \
"v_min_u32 v174, -2, v174\n" \
"v_min_u32 v175, -2, v175\n" \
"global_store_dwordx4 v0, v[172:175], s[52:53] offset:0 sc1\n" \
"v_mfma_f32_16x16x16_bf16 v[160:163], v[102:103], v[74:75], v[160:163]\n" \
"v_pk_mul_f32 v[168:169], v[168:169], v[190:191] op_sel_hi:[1,0]\n" \
"v_pk_mul_f32 v[170:171], v[170:171], v[190:191] op_sel_hi:[1,0]\n" \
"v_cvt_pk_bf16_f32 v176, v148, v149\n" \
"v_cvt_pk_bf16_f32 v177, v150, v151\n" \
"v_mfma_f32_16x16x16_bf16 v[164:167], v[104:105], v[74:75], v[164:167]\n" \
"v_cvt_pk_bf16_f32 v178, v152, v153\n" \
"v_cvt_pk_bf16_f32 v179, v154, v155\n" \
"v_min_u32 v176, -2, v176\n" \
"v_min_u32 v177, -2, v177\n" \
"v_min_u32 v178, -2, v178\n" \
"v_min_u32 v179, -2, v179\n" \
"global_store_dwordx4 v0, v[176:179], s[52:53] offset:1024 sc1\n" \
"v_mfma_f32_16x16x16_bf16 v[168:171], v[106:107], v[74:75], v[168:171]\n" \
"v_cvt_pk_bf16_f32 v180, v156, v157\n" \
"v_cvt_pk_bf16_f32 v181, v158, v159\n" \
"v_cvt_pk_bf16_f32 v182, v160, v161\n" \
"v_cvt_pk_bf16_f32 v183, v162, v163\n" \
"v_min_u32 v180, -2, v180\n" \
"v_min_u32 v181, -2, v181\n" \
"v_min_u32 v182, -2, v182\n" \
"v_min_u32 v183, -2, v183\n" \
"global_store_dwordx4 v0, v[180:183], s[52:53] offset:2048 sc1\n" \
"s_nop 3\n" \
"v_cvt_pk_bf16_f32 v184, v164, v165\n" \
"v_cvt_pk_bf16_f32 v185, v166, v167\n" \
"s_nop 3\n" \
"v_cvt_pk_bf16_f32 v186, v168, v169\n" \
"v_cvt_pk_bf16_f32 v187, v170, v171\n" \
"v_min_u32 v184, -2, v184\n" \
"v_min_u32 v185, -2, v185\n" \
"v_min_u32 v186, -2, v186\n" \
"v_min_u32 v187, -2, v187\n" \
"global_store_dwordx4 v0, v[184:187], s[52:53] offset:3072 sc1\n" \
"s_nop 3\n" \
"s_add_u32 s61, s56, 1\n" \
"v_mov_b32 v13, s61\n" \
"s_add_u32 s50, s50, 0x80000\n" \
"s_addc_u32 s51, s51, 0\n" \
"s_add_u32 s52, s52, 0x80000\n" \
"s_addc_u32 s53, s53, 0\n" \
"s_add_u32 s56, s56, 2\n" \
"s_cmp_ge_u32 s56, 64\n" \
"s_cbranch_scc1 Lt_end%=\n" \
"s_branch Lt0_top%=\n" \
"Lt0_t1%=:\n" \
"s_add_u32 s62, s61, 1\n" \
"s_mov_b32 s60, 0\n" \
"Lt0_t1p%=:\n" \
"global_load_dword v6, v4, s[54:55] sc1\n" \
"s_add_u32 s60, s60, 1\n" \
"s_waitcnt vmcnt(0)\n" \
"v_cmp_gt_u32 vcc, s62, v6\n" \
"s_cbranch_vccz Lt0_t1p_ok%=\n" \
"s_sleep 4\n" \
"s_cmp_lt_u32 s60, 262144\n" \
"s_cbranch_scc1 Lt0_t1p%=\n" \
"s_branch Lt_fail%=\n" \
"Lt0_t1p_ok%=:\n" \
"s_nop 1\n" \
"v_min_u32_dpp v6, v6, v6 row_ror:8 row_mask:0xf bank_mask:0xf\n" \
"s_nop 1\n" \
"v_min_u32_dpp v6, v6, v6 row_ror:4 row_mask:0xf bank_mask:0xf\n" \
"s_nop 1\n" \
"v_min_u32_dpp v6, v6, v6 row_ror:2 row_mask:0xf bank_mask:0xf\n" \
"s_nop 1\n" \
"v_min_u32_dpp v6, v6, v6 row_ror:1 row_mask:0xf bank_mask:0xf\n" \
"s_nop 1\n" \
"v_readfirstlane_b32 s57, v6\n" \
"s_branch Lt0_t1ok%=\n" \
"Lt0_w0%=:\n" \
"s_waitcnt vmcnt(0)\n" \
"s_branch Lt0_w1%=\n" \
"Lt0_pr%=:\n" \
"s_mov_b32 s60, 0\n" \
"Lt0_prp%=:\n" \
"global_load_dword v7, v5, s[54:55] sc1\n" \
"s_add_u32 s60, s60, 1\n" \
"s_waitcnt vmcnt(0)\n" \
"v_cmp_gt_u32 vcc, s61, v7\n" \
"s_cbranch_vccz Lt0_prok%=\n" \
"s_cmp_lt_u32 s60, 262144\n" \
"s_cbranch_scc1 Lt0_prp%=\n" \
"s_branch Lt_fail%=\n" \
"Lt1_t1%=:\n" \
"s_add_u32 s62, s61, 1\n" \
"s_mov_b32 s60, 0\n" \
"Lt1_t1p%=:\n" \
"global_load_dword v6, v4, s[54:55] sc1\n" \
"s_add_u32 s60, s60, 1\n" \
"s_waitcnt vmcnt(0)\n" \
"v_cmp_gt_u32 vcc, s62, v6\n" \
"s_cbranch_vccz Lt1_t1p_ok%=\n" \
"s_sleep 4\n" \
"s_cmp_lt_u32 s60, 262144\n" \
"s_cbranch_scc1 Lt1_t1p%=\n" \
"s_branch Lt_fail%=\n" \
"Lt1_t1p_ok%=:\n" \
"s_nop 1\n" \
"v_min_u32_dpp v6, v6, v6 row_ror:8 row_mask:0xf bank_mask:0xf\n" \
"s_nop 1\n" \
"v_min_u32_dpp v6, v6, v6 row_ror:4 row_mask:0xf bank_mask:0xf\n" \
"s_nop 1\n" \
"v_min_u32_dpp v6, v6, v6 row_ror:2 row_mask:0xf bank_mask:0xf\n" \
"s_nop 1\n" \
"v_min_u32_dpp v6, v6, v6 row_ror:1 row_mask:0xf bank_mask:0xf\n" \
"s_nop 1\n" \
"v_readfirstlane_b32 s57, v6\n" \
"s_branch Lt1_t1ok%=\n" \
"Lt1_w0%=:\n" \
"s_waitcnt vmcnt(0)\n" \
"s_branch Lt1_w1%=\n" \
"Lt1_pr%=:\n" \
"s_mov_b32 s60, 0\n" \
"Lt1_prp%=:\n" \
"global_load_dword v7, v5, s[54:55] sc1\n" \
"s_add_u32 s60, s60, 1\n" \
"s_waitcnt vmcnt(0)\n" \
"v_cmp_gt_u32 vcc, s61, v7\n" \
"s_cbranch_vccz Lt1_prok%=\n" \
"s_cmp_lt_u32 s60, 262144\n" \
"s_cbranch_scc1 Lt1_prp%=\n" \
"s_branch Lt_fail%=\n" \
"Lt_last%=:\n" \
"s_mov_b32 s60, 0\n" \
"Lt_last2%=:\n" \
"global_load_dwordx4 v[76:79], v2, s[46:47] offset:0 sc1\n" \
"global_load_dwordx4 v[80:83], v2, s[46:47] offset:512 sc1\n" \
"global_load_dwordx4 v[84:87], v2, s[46:47] offset:1024 sc1\n" \
"global_load_dwordx4 v[88:91], v2, s[46:47] offset:1536 sc1\n" \
"global_load_dwordx2 v[92:93], v3, s[48:49] offset:0 sc1\n" \
"global_load_dwordx2 v[94:95], v3, s[48:49] offset:256 sc1\n" \
"global_load_dwordx2 v[96:97], v3, s[48:49] offset:512 sc1\n" \
"global_load_dwordx2 v[98:99], v3, s[48:49] offset:768 sc1\n" \
"global_load_dwordx2 v[100:101], v3, s[48:49] offset:1024 sc1\n" \
"global_load_dwordx2 v[102:103], v3, s[48:49] offset:1280 sc1\n" \
"global_load_dwordx2 v[104:105], v3, s[48:49] offset:1536 sc1\n" \
"global_load_dwordx2 v[106:107], v3, s[48:49] offset:1792 sc1\n" \
"s_add_u32 s60, s60, 1\n" \
"s_waitcnt vmcnt(0)\n" \
"v_max3_u32 v8, v76, v77, v78\n" \
"v_max3_u32 v8, v8, v79, v80\n" \
"v_max3_u32 v8, v8, v81, v82\n" \
"v_max3_u32 v8, v8, v83, v84\n" \
"v_max3_u32 v8, v8, v85, v86\n" \
"v_max3_u32 v8, v8, v87, v88\n" \
"v_max3_u32 v8, v8, v89, v90\n" \
"v_max3_u32 v8, v8, v91, v92\n" \
"v_max3_u32 v8, v8, v93, v94\n" \
"v_max3_u32 v8, v8, v95, v96\n" \
"v_max3_u32 v8, v8, v97, v98\n" \
"v_max3_u32 v8, v8, v99, v100\n" \
"v_max3_u32 v8, v8, v101, v102\n" \
"v_max3_u32 v8, v8, v103, v104\n" \
"v_max3_u32 v8, v8, v105, v106\n" \
"v_max_u32 v8, v8, v107\n" \
"v_cmp_eq_u32 vcc, -1, v8\n" \
"s_cbranch_vccz Lt_lastok%=\n" \
"s_cmp_lt_u32 s60, 262144\n" \
"s_cbranch_scc1 Lt_last2%=\n" \
"s_branch Lt_fail%=\n" \
"Lt_fail%=:\n" \
"s_mov_b64 exec, 1\n" \
"v_mov_b32 v8, 16448\n" \
"v_mov_b32 v9, 4\n" \
"global_store_dword v8, v9, s[54:55] sc1\n" \
"s_mov_b64 exec, -1\n" \
"s_branch Lt_done%=\n" \
"Lt_end%=:\n" \
"s_cmp_eq_u32 s56, 65\n" \
"s_cbranch_scc1 Lt_nw%=\n" \
"s_waitcnt vmcnt(0)\n" \
"Lt_nw%=:\n" \
"s_mov_b64 exec, 1\n" \
"global_store_dword v15, v13, s[54:55] sc1\n" \
"s_mov_b64 exec, -1\n" \
"Lt_done%=:\n" \
"s_waitcnt vmcnt(0)\n" \
""
#define T2_CLOB "v0", "v1", "v2", "v3", "v4", "v5", "v6", "v7", "v8", "v9", "v10", "v11", "v12", "v13", "v14", "v15", "v16", "v17", "v18", "v19", "v20", "v21", "v22", "v23", "v24", "v25", "v26", "v27", "v28", "v29", "v30", "v31", "v32", "v33", "v34", "v35", "v36", "v37", "v38", "v39", "v40", "v41", "v42", "v43", "v44", "v45", "v46", "v47", "v48", "v49", "v50", "v51", "v52", "v53", "v54", "v55", "v56", "v57", "v58", "v59", "v60", "v61", "v62", "v63", "v64", "v65", "v66", "v67", "v68", "v69", "v70", "v71", "v72", "v73", "v74", "v75", "v76", "v77", "v78", "v79", "v80", "v81", "v82", "v83", "v84", "v85", "v86", "v87", "v88", "v89", "v90", "v91", "v92", "v93", "v94", "v95", "v96", "v97", "v98", "v99", "v100", "v101", "v102", "v103", "v104", "v105", "v106", "v107", "v108", "v109", "v110", "v111", "v112", "v113", "v114", "v115", "v116", "v117", "v118", "v119", "v120", "v121", "v122", "v123", "v124", "v125", "v126", "v127", "v128", "v129", "v130", "v131", "v132", "v133", "v134", "v135", "v136", "v137", "v138", "v139", "v140", "v141", "v142", "v143", "v144", "v145", "v146", "v147", "v148", "v149", "v150", "v151", "v152", "v153", "v154", "v155", "v156", "v157", "v158", "v159", "v160", "v161", "v162", "v163", "v164", "v165", "v166", "v167", "v168", "v169", "v170", "v171", "v172", "v173", "v174", "v175", "v176", "v177", "v178", "v179", "v180", "v181", "v182", "v183", "v184", "v185", "v186", "v187", "v188", "v189", "v190", "v191", "v192", "v193", "v194", "v195", "v196", "v197", "v198", "v199", "a0", "a1", "a2", "a3", "a4", "a5", "a6", "a7", "a8", "a9", "a10", "a11", "a12", "a13", "a14", "a15", "a16", "a17", "a18", "a19", "a20", "a21", "a22", "a23", "a24", "a25", "a26", "a27", "a28", "a29", "a30", "a31", "a32", "a33", "a34", "a35", "a36", "a37", "a38", "a39", "a40", "a41", "a42", "a43", "a44", "a45", "a46", "a47", "a48", "a49", "a50", "a51", "a52", "a53", "a54", "a55", "a56", "a57", "a58", "a59", "a60", "a61", "a62", "a63", "a64", "a65", "a66", "a67", "a68", "a69", "a70", "a71", "a72", "a73", "a74", "a75", "a76", "a77", "a78", "a79", "a80", "a81", "a82", "a83", "a84", "a85", "a86", "a87", "a88", "a89", "a90", "a91", "a92", "a93", "a94", "a95", "a96", "a97", "a98", "a99", "a100", "a101", "a102", "a103", "a104", "a105", "a106", "a107", "a108", "a109", "a110", "a111", "a112", "a113", "a114", "a115", "a116", "a117", "a118", "a119", "a120", "a121", "a122", "a123", "a124", "a125", "a126", "a127", "a128", "a129", "a130", "a131", "a132", "a133", "a134", "a135", "a136", "a137", "a138", "a139", "a140", "a141", "a142", "a143", "a144", "a145", "a146", "a147", "a148", "a149", "a150", "a151", "a152", "a153", "a154", "a155", "a156", "a157", "a158", "a159", "a160", "a161", "a162", "a163", "a164", "a165", "a166", "a167", "a168", "a169", "a170", "a171", "a172", "a173", "a174", "a175", "a176", "a177", "a178", "a179", "a180", "a181", "a182", "a183", "a184", "a185", "a186", "a187", "a188", "a189", "a190", "a191", "s40", "s41", "s42", "s43", "s44", "s45", "s46", "s47", "s48", "s49", "s50", "s51", "s52", "s53", "s54", "s55", "s56", "s57", "s58", "s59", "s60", "s61", "s62", "s63", "s64", "s65", "s66", "s67", "s68", "s69", "s70", "s71", "vcc", "scc", "memory"
#define T3_ASM \
"s_mov_b64 s[40:41], %0\n" \
"s_mov_b64 s[42:43], %1\n" \
"s_mov_b64 s[46:47], %2\n" \
"s_mov_b64 s[48:49], %3\n" \
"s_mov_b32 s50, %5\n" \
"s_mov_b32 s52, %6\n" \
"s_add_u32 s44, s42, 0x80000\n" \
"s_addc_u32 s45, s43, 0\n" \
"s_mov_b32 s51, 0\n" \
"s_mov_b32 s53, 0\n" \
"v_mbcnt_lo_u32_b32 v21, -1, 0\n" \
"v_mbcnt_hi_u32_b32 v21, -1, v21\n" \
"v_lshlrev_b32 v0, 4, v21\n" \
"v_lshrrev_b32 v22, 2, v21\n" \
"v_and_b32 v23, 3, v21\n" \
"v_mul_u32_u24 v60, 1280, v22\n" \
"v_lshl_add_u32 v60, v23, 4, v60\n" \
"s_lshl_b32 s55, s52, 15\n" \
"s_mov_b32 s56, s55\n" \
"s_mov_b32 s54, 16\n" \
"Lu_st0%=:\n" \
"s_mov_b32 m0, s56\n" \
"s_add_u32 s56, s56, 1024\n" \
"global_load_lds_dwordx4 v60, s[40:41]\n" \
"s_add_u32 s40, s40, 20480\n" \
"s_addc_u32 s41, s41, 0\n" \
"s_sub_u32 s54, s54, 1\n" \
"s_waitcnt vmcnt(8)\n" \
"s_cmp_lg_u32 s54, 0\n" \
"s_cbranch_scc1 Lu_st0%=\n" \
"v_lshrrev_b32 v24, 4, v21\n" \
"v_and_b32 v25, 3, v22\n" \
"v_lshl_add_u32 v25, v24, 2, v25\n" \
"v_lshlrev_b32 v25, 6, v25\n" \
"v_lshl_add_u32 v25, v23, 4, v25\n" \
"v_add_u32 v61, s55, v25\n" \
"v_and_b32 v25, 15, v21\n" \
"v_mul_u32_u24 v18, 2560, v25\n" \
"v_lshl_add_u32 v18, v24, 4, v18\n" \
"v_and_b32 v25, 7, v21\n" \
"v_lshlrev_b32 v25, 2, v25\n" \
"v_add_u32 v16, %4, v25\n" \
"s_lshl_b32 s54, s52, 11\n" \
"s_add_u32 s54, s54, 131072\n" \
"v_add_u32 v19, s54, v0\n" \
"v_add_u32 v20, 131072, v0\n" \
"v_add_u32 v1, 4096, v0\n" \
"v_add_u32 v2, 8192, v0\n" \
"v_add_u32 v3, 12288, v0\n" \
"v_add_u32 v4, 16384, v0\n" \
"v_add_u32 v5, 20480, v0\n" \
"v_add_u32 v6, 24576, v0\n" \
"v_add_u32 v7, 28672, v0\n" \
"s_mov_b32 s54, 16\n" \
"Lu_st1%=:\n" \
"s_mov_b32 m0, s56\n" \
"s_add_u32 s56, s56, 1024\n" \
"global_load_lds_dwordx4 v60, s[40:41]\n" \
"s_add_u32 s40, s40, 20480\n" \
"s_addc_u32 s41, s41, 0\n" \
"s_sub_u32 s54, s54, 1\n" \
"s_waitcnt vmcnt(8)\n" \
"s_cmp_lg_u32 s54, 0\n" \
"s_cbranch_scc1 Lu_st1%=\n" \
"s_mov_b32 s56, s55\n" \
"ds_read_b64_tr_b16 a[0:1], v61 offset:0\n" \
"ds_read_b64_tr_b16 a[2:3], v61 offset:1024\n" \
"ds_read_b64_tr_b16 a[128:129], v61 offset:8\n" \
"ds_read_b64_tr_b16 a[130:131], v61 offset:1032\n" \
"ds_read_b64_tr_b16 a[4:5], v61 offset:2048\n" \
"ds_read_b64_tr_b16 a[6:7], v61 offset:3072\n" \
"ds_read_b64_tr_b16 a[132:133], v61 offset:2056\n" \
"ds_read_b64_tr_b16 a[134:135], v61 offset:3080\n" \
"ds_read_b64_tr_b16 a[8:9], v61 offset:4096\n" \
"ds_read_b64_tr_b16 a[10:11], v61 offset:5120\n" \
"ds_read_b64_tr_b16 a[136:137], v61 offset:4104\n" \
"ds_read_b64_tr_b16 a[138:139], v61 offset:5128\n" \
"ds_read_b64_tr_b16 a[12:13], v61 offset:6144\n" \
"ds_read_b64_tr_b16 a[14:15], v61 offset:7168\n" \
"ds_read_b64_tr_b16 a[140:141], v61 offset:6152\n" \
"ds_read_b64_tr_b16 a[142:143], v61 offset:7176\n" \
"ds_read_b64_tr_b16 a[16:17], v61 offset:8192\n" \
"ds_read_b64_tr_b16 a[18:19], v61 offset:9216\n" \
"ds_read_b64_tr_b16 a[144:145], v61 offset:8200\n" \
"ds_read_b64_tr_b16 a[146:147], v61 offset:9224\n" \
"ds_read_b64_tr_b16 a[20:21], v61 offset:10240\n" \
"ds_read_b64_tr_b16 a[22:23], v61 offset:11264\n" \
"ds_read_b64_tr_b16 a[148:149], v61 offset:10248\n" \
"ds_read_b64_tr_b16 a[150:151], v61 offset:11272\n" \
"ds_read_b64_tr_b16 a[24:25], v61 offset:12288\n" \
"ds_read_b64_tr_b16 a[26:27], v61 offset:13312\n" \
"ds_read_b64_tr_b16 a[152:153], v61 offset:12296\n" \
"ds_read_b64_tr_b16 a[154:155], v61 offset:13320\n" \
"ds_read_b64_tr_b16 a[28:29], v61 offset:14336\n" \
"ds_read_b64_tr_b16 a[30:31], v61 offset:15360\n" \
"ds_read_b64_tr_b16 a[156:157], v61 offset:14344\n" \
"ds_read_b64_tr_b16 a[158:159], v61 offset:15368\n" \
"s_waitcnt lgkmcnt(0)\n" \
"s_mov_b32 s54, 16\n" \
"Lu_st2%=:\n" \
"s_mov_b32 m0, s56\n" \
"s_add_u32 s56, s56, 1024\n" \
"global_load_lds_dwordx4 v60, s[40:41]\n" \
"s_add_u32 s40, s40, 20480\n" \
"s_addc_u32 s41, s41, 0\n" \
"s_sub_u32 s54, s54, 1\n" \
"s_waitcnt vmcnt(8)\n" \
"s_cmp_lg_u32 s54, 0\n" \
"s_cbranch_scc1 Lu_st2%=\n" \
"ds_read_b64_tr_b16 a[32:33], v61 offset:16384\n" \
"ds_read_b64_tr_b16 a[34:35], v61 offset:17408\n" \
"ds_read_b64_tr_b16 a[160:161], v61 offset:16392\n" \
"ds_read_b64_tr_b16 a[162:163], v61 offset:17416\n" \
"ds_read_b64_tr_b16 a[36:37], v61 offset:18432\n" \
"ds_read_b64_tr_b16 a[38:39], v61 offset:19456\n" \
"ds_read_b64_tr_b16 a[164:165], v61 offset:18440\n" \
"ds_read_b64_tr_b16 a[166:167], v61 offset:19464\n" \
"ds_read_b64_tr_b16 a[40:41], v61 offset:20480\n" \
"ds_read_b64_tr_b16 a[42:43], v61 offset:21504\n" \
"ds_read_b64_tr_b16 a[168:169], v61 offset:20488\n" \
"ds_read_b64_tr_b16 a[170:171], v61 offset:21512\n" \
"ds_read_b64_tr_b16 a[44:45], v61 offset:22528\n" \
"ds_read_b64_tr_b16 a[46:47], v61 offset:23552\n" \
"ds_read_b64_tr_b16 a[172:173], v61 offset:22536\n" \
"ds_read_b64_tr_b16 a[174:175], v61 offset:23560\n" \
"ds_read_b64_tr_b16 a[48:49], v61 offset:24576\n" \
"ds_read_b64_tr_b16 a[50:51], v61 offset:25600\n" \
"ds_read_b64_tr_b16 a[176:177], v61 offset:24584\n" \
"ds_read_b64_tr_b16 a[178:179], v61 offset:25608\n" \
"ds_read_b64_tr_b16 a[52:53], v61 offset:26624\n" \
"ds_read_b64_tr_b16 a[54:55], v61 offset:27648\n" \
"ds_read_b64_tr_b16 a[180:181], v61 offset:26632\n" \
"ds_read_b64_tr_b16 a[182:183], v61 offset:27656\n" \
"ds_read_b64_tr_b16 a[56:57], v61 offset:28672\n" \
"ds_read_b64_tr_b16 a[58:59], v61 offset:29696\n" \
"ds_read_b64_tr_b16 a[184:185], v61 offset:28680\n" \
"ds_read_b64_tr_b16 a[186:187], v61 offset:29704\n" \
"ds_read_b64_tr_b16 a[60:61], v61 offset:30720\n" \
"ds_read_b64_tr_b16 a[62:63], v61 offset:31744\n" \
"ds_read_b64_tr_b16 a[188:189], v61 offset:30728\n" \
"ds_read_b64_tr_b16 a[190:191], v61 offset:31752\n" \
"s_waitcnt lgkmcnt(0)\n" \
"s_mov_b32 s54, 16\n" \
"Lu_st3%=:\n" \
"s_mov_b32 m0, s56\n" \
"s_add_u32 s56, s56, 1024\n" \
"global_load_lds_dwordx4 v60, s[40:41]\n" \
"s_add_u32 s40, s40, 20480\n" \
"s_addc_u32 s41, s41, 0\n" \
"s_sub_u32 s54, s54, 1\n" \
"s_waitcnt vmcnt(8)\n" \
"s_cmp_lg_u32 s54, 0\n" \
"s_cbranch_scc1 Lu_st3%=\n" \
"s_mov_b32 s56, s55\n" \
"ds_read_b64_tr_b16 a[64:65], v61 offset:0\n" \
"ds_read_b64_tr_b16 a[66:67], v61 offset:1024\n" \
"ds_read_b64_tr_b16 a[192:193], v61 offset:8\n" \
"ds_read_b64_tr_b16 a[194:195], v61 offset:1032\n" \
"ds_read_b64_tr_b16 a[68:69], v61 offset:2048\n" \
"ds_read_b64_tr_b16 a[70:71], v61 offset:3072\n" \
"ds_read_b64_tr_b16 a[196:197], v61 offset:2056\n" \
"ds_read_b64_tr_b16 a[198:199], v61 offset:3080\n" \
"ds_read_b64_tr_b16 a[72:73], v61 offset:4096\n" \
"ds_read_b64_tr_b16 a[74:75], v61 offset:5120\n" \
"ds_read_b64_tr_b16 a[200:201], v61 offset:4104\n" \
"ds_read_b64_tr_b16 a[202:203], v61 offset:5128\n" \
"ds_read_b64_tr_b16 a[76:77], v61 offset:6144\n" \
"ds_read_b64_tr_b16 a[78:79], v61 offset:7168\n" \
"ds_read_b64_tr_b16 a[204:205], v61 offset:6152\n" \
"ds_read_b64_tr_b16 a[206:207], v61 offset:7176\n" \
"ds_read_b64_tr_b16 a[80:81], v61 offset:8192\n" \
"ds_read_b64_tr_b16 a[82:83], v61 offset:9216\n" \
"ds_read_b64_tr_b16 a[208:209], v61 offset:8200\n" \
"ds_read_b64_tr_b16 a[210:211], v61 offset:9224\n" \
"ds_read_b64_tr_b16 a[84:85], v61 offset:10240\n" \
"ds_read_b64_tr_b16 a[86:87], v61 offset:11264\n" \
"ds_read_b64_tr_b16 a[212:213], v61 offset:10248\n" \
"ds_read_b64_tr_b16 a[214:215], v61 offset:11272\n" \
"ds_read_b64_tr_b16 a[88:89], v61 offset:12288\n" \
"ds_read_b64_tr_b16 a[90:91], v61 offset:13312\n" \
"ds_read_b64_tr_b16 a[216:217], v61 offset:12296\n" \
"ds_read_b64_tr_b16 a[218:219], v61 offset:13320\n" \
"ds_read_b64_tr_b16 a[92:93], v61 offset:14336\n" \
"ds_read_b64_tr_b16 a[94:95], v61 offset:15360\n" \
"ds_read_b64_tr_b16 a[220:221], v61 offset:14344\n" \
"ds_read_b64_tr_b16 a[222:223], v61 offset:15368\n" \
"s_waitcnt lgkmcnt(0)\n" \
"s_waitcnt vmcnt(0)\n" \
"ds_read_b64_tr_b16 a[96:97], v61 offset:16384\n" \
"ds_read_b64_tr_b16 a[98:99], v61 offset:17408\n" \
"ds_read_b64_tr_b16 a[224:225], v61 offset:16392\n" \
"ds_read_b64_tr_b16 a[226:227], v61 offset:17416\n" \
"ds_read_b64_tr_b16 a[100:101], v61 offset:18432\n" \
"ds_read_b64_tr_b16 a[102:103], v61 offset:19456\n" \
"ds_read_b64_tr_b16 a[228:229], v61 offset:18440\n" \
"ds_read_b64_tr_b16 a[230:231], v61 offset:19464\n" \
"ds_read_b64_tr_b16 a[104:105], v61 offset:20480\n" \
"ds_read_b64_tr_b16 a[106:107], v61 offset:21504\n" \
"ds_read_b64_tr_b16 a[232:233], v61 offset:20488\n" \
"ds_read_b64_tr_b16 a[234:235], v61 offset:21512\n" \
"ds_read_b64_tr_b16 a[108:109], v61 offset:22528\n" \
"ds_read_b64_tr_b16 a[110:111], v61 offset:23552\n" \
"ds_read_b64_tr_b16 a[236:237], v61 offset:22536\n" \
"ds_read_b64_tr_b16 a[238:239], v61 offset:23560\n" \
"ds_read_b64_tr_b16 a[112:113], v61 offset:24576\n" \
"ds_read_b64_tr_b16 a[114:115], v61 offset:25600\n" \
"ds_read_b64_tr_b16 a[240:241], v61 offset:24584\n" \
"ds_read_b64_tr_b16 a[242:243], v61 offset:25608\n" \
"ds_read_b64_tr_b16 a[116:117], v61 offset:26624\n" \
"ds_read_b64_tr_b16 a[118:119], v61 offset:27648\n" \
"ds_read_b64_tr_b16 a[244:245], v61 offset:26632\n" \
"ds_read_b64_tr_b16 a[246:247], v61 offset:27656\n" \
"ds_read_b64_tr_b16 a[120:121], v61 offset:28672\n" \
"ds_read_b64_tr_b16 a[122:123], v61 offset:29696\n" \
"ds_read_b64_tr_b16 a[248:249], v61 offset:28680\n" \
"ds_read_b64_tr_b16 a[250:251], v61 offset:29704\n" \
"ds_read_b64_tr_b16 a[124:125], v61 offset:30720\n" \
"ds_read_b64_tr_b16 a[126:127], v61 offset:31744\n" \
"ds_read_b64_tr_b16 a[252:253], v61 offset:30728\n" \
"ds_read_b64_tr_b16 a[254:255], v61 offset:31752\n" \
"s_waitcnt lgkmcnt(0)\n" \
"Lu_fresh%=:\n" \
"s_add_u32 s55, s50, 1\n" \
"s_cmp_ge_u32 s51, s55\n" \
"s_cbranch_scc1 Lu_have%=\n" \
"s_mov_b32 s54, 0\n" \
"Lu_poll%=:\n" \
"global_load_dword v17, v16, s[48:49] sc1\n" \
"s_add_u32 s54, s54, 1\n" \
"s_waitcnt vmcnt(0)\n" \
"v_cmp_gt_u32 vcc, s55, v17\n" \
"s_cbranch_vccz Lu_pollok%=\n" \
"s_cmp_lt_u32 s54, 262144\n" \
"s_cbranch_scc1 Lu_poll%=\n" \
"s_branch Lu_fail%=\n" \
"Lu_pollok%=:\n" \
"s_nop 1\n" \
"v_min_u32_dpp v17, v17, v17 row_ror:4 row_mask:0xf bank_mask:0xf\n" \
"s_nop 1\n" \
"v_min_u32_dpp v17, v17, v17 row_ror:2 row_mask:0xf bank_mask:0xf\n" \
"s_nop 1\n" \
"v_min_u32_dpp v17, v17, v17 row_ror:1 row_mask:0xf bank_mask:0xf\n" \
"s_nop 1\n" \
"v_readfirstlane_b32 s51, v17\n" \
"Lu_have%=:\n" \
"global_load_dwordx4 v[128:131], v0, s[42:43] offset:0 sc1\n" \
"global_load_dwordx4 v[132:135], v0, s[42:43] offset:1024 sc1\n" \
"global_load_dwordx4 v[136:139], v0, s[42:43] offset:2048 sc1\n" \
"global_load_dwordx4 v[140:143], v0, s[42:43] offset:3072 sc1\n" \
"global_load_dwordx4 v[144:147], v1, s[42:43] offset:0 sc1\n" \
"global_load_dwordx4 v[148:151], v1, s[42:43] offset:1024 sc1\n" \
"global_load_dwordx4 v[152:155], v1, s[42:43] offset:2048 sc1\n" \
"global_load_dwordx4 v[156:159], v1, s[42:43] offset:3072 sc1\n" \
"global_load_dwordx4 v[160:163], v2, s[42:43] offset:0 sc1\n" \
"global_load_dwordx4 v[164:167], v2, s[42:43] offset:1024 sc1\n" \
"global_load_dwordx4 v[168:171], v2, s[42:43] offset:2048 sc1\n" \
"global_load_dwordx4 v[172:175], v2, s[42:43] offset:3072 sc1\n" \
"global_load_dwordx4 v[176:179], v3, s[42:43] offset:0 sc1\n" \
"global_load_dwordx4 v[180:183], v3, s[42:43] offset:1024 sc1\n" \
"global_load_dwordx4 v[184:187], v3, s[42:43] offset:2048 sc1\n" \
"global_load_dwordx4 v[188:191], v3, s[42:43] offset:3072 sc1\n" \
"global_load_dwordx4 v[192:195], v4, s[42:43] offset:0 sc1\n" \
"global_load_dwordx4 v[196:199], v4, s[42:43] offset:1024 sc1\n" \
"global_load_dwordx4 v[200:203], v4, s[42:43] offset:2048 sc1\n" \
"global_load_dwordx4 v[204:207], v4, s[42:43] offset:3072 sc1\n" \
"global_load_dwordx4 v[208:211], v5, s[42:43] offset:0 sc1\n" \
"global_load_dwordx4 v[212:215], v5, s[42:43] offset:1024 sc1\n" \
"global_load_dwordx4 v[216:219], v5, s[42:43] offset:2048 sc1\n" \
"global_load_dwordx4 v[220:223], v5, s[42:43] offset:3072 sc1\n" \
"global_load_dwordx4 v[224:227], v6, s[42:43] offset:0 sc1\n" \
"global_load_dwordx4 v[228:231], v6, s[42:43] offset:1024 sc1\n" \
"global_load_dwordx4 v[232:235], v6, s[42:43] offset:2048 sc1\n" \
"global_load_dwordx4 v[236:239], v6, s[42:43] offset:3072 sc1\n" \
"global_load_dwordx4 v[240:243], v7, s[42:43] offset:0 sc1\n" \
"global_load_dwordx4 v[244:247], v7, s[42:43] offset:1024 sc1\n" \
"global_load_dwordx4 v[248:251], v7, s[42:43] offset:2048 sc1\n" \
"global_load_dwordx4 v[252:255], v7, s[42:43] offset:3072 sc1\n" \
"Lu_top%=:\n" \
"s_add_u32 s55, s50, 3\n" \
"s_cmp_gt_u32 s55, 64\n" \
"s_cbranch_scc1 Lu_drain%=\n" \
"s_cmp_ge_u32 s51, s55\n" \
"s_cbranch_scc0 Lu_drain%=\n" \
"s_branch Lu_drain%=\n" \
"global_load_dword v17, v16, s[48:49] sc1\n" \
"s_waitcnt vmcnt(32)\n" \
"v_mfma_f32_16x16x32_bf16 v[8:11], a[0:3], v[128:131], 0\n" \
"v_mfma_f32_16x16x32_bf16 v[12:15], a[128:131], v[128:131], 0\n" \
"global_load_dwordx4 v[128:131], v0, s[44:45] offset:0 sc1\n" \
"s_waitcnt vmcnt(32)\n" \
"v_mfma_f32_16x16x32_bf16 v[8:11], a[4:7], v[132:135], v[8:11]\n" \
"v_mfma_f32_16x16x32_bf16 v[12:15], a[132:135], v[132:135], v[12:15]\n" \
"global_load_dwordx4 v[132:135], v0, s[44:45] offset:1024 sc1\n" \
"s_waitcnt vmcnt(32)\n" \
"v_mfma_f32_16x16x32_bf16 v[8:11], a[8:11], v[136:139], v[8:11]\n" \
"v_mfma_f32_16x16x32_bf16 v[12:15], a[136:139], v[136:139], v[12:15]\n" \
"global_load_dwordx4 v[136:139], v0, s[44:45] offset:2048 sc1\n" \
"s_waitcnt vmcnt(32)\n" \
"v_mfma_f32_16x16x32_bf16 v[8:11], a[12:15], v[140:143], v[8:11]\n" \
"v_mfma_f32_16x16x32_bf16 v[12:15], a[140:143], v[140:143], v[12:15]\n" \
"global_load_dwordx4 v[140:143], v0, s[44:45] offset:3072 sc1\n" \
"s_waitcnt vmcnt(32)\n" \
"v_mfma_f32_16x16x32_bf16 v[8:11], a[16:19], v[144:147], v[8:11]\n" \
"v_mfma_f32_16x16x32_bf16 v[12:15], a[144:147], v[144:147], v[12:15]\n" \
"global_load_dwordx4 v[144:147], v1, s[44:45] offset:0 sc1\n" \
"s_waitcnt vmcnt(32)\n" \
"v_mfma_f32_16x16x32_bf16 v[8:11], a[20:23], v[148:151], v[8:11]\n" \
"v_mfma_f32_16x16x32_bf16 v[12:15], a[148:151], v[148:151], v[12:15]\n" \
"global_load_dwordx4 v[148:151], v1, s[44:45] offset:1024 sc1\n" \
"s_waitcnt vmcnt(32)\n" \
"v_mfma_f32_16x16x32_bf16 v[8:11], a[24:27], v[152:155], v[8:11]\n" \
"v_mfma_f32_16x16x32_bf16 v[12:15], a[152:155], v[152:155], v[12:15]\n" \
"global_load_dwordx4 v[152:155], v1, s[44:45] offset:2048 sc1\n" \
"s_waitcnt vmcnt(32)\n" \
"v_mfma_f32_16x16x32_bf16 v[8:11], a[28:31], v[156:159], v[8:11]\n" \
"v_mfma_f32_16x16x32_bf16 v[12:15], a[156:159], v[156:159], v[12:15]\n" \
"global_load_dwordx4 v[156:159], v1, s[44:45] offset:3072 sc1\n" \
"s_waitcnt vmcnt(32)\n" \
"v_mfma_f32_16x16x32_bf16 v[8:11], a[32:35], v[160:163], v[8:11]\n" \
"v_mfma_f32_16x16x32_bf16 v[12:15], a[160:163], v[160:163], v[12:15]\n" \
"global_load_dwordx4 v[160:163], v2, s[44:45] offset:0 sc1\n" \
"s_waitcnt vmcnt(32)\n" \
"v_mfma_f32_16x16x32_bf16 v[8:11], a[36:39], v[164:167], v[8:11]\n" \
"v_mfma_f32_16x16x32_bf16 v[12:15], a[164:167], v[164:167], v[12:15]\n" \
"global_load_dwordx4 v[164:167], v2, s[44:45] offset:1024 sc1\n" \
"s_waitcnt vmcnt(32)\n" \
"v_mfma_f32_16x16x32_bf16 v[8:11], a[40:43], v[168:171], v[8:11]\n" \
"v_mfma_f32_16x16x32_bf16 v[12:15], a[168:171], v[168:171], v[12:15]\n" \
"global_load_dwordx4 v[168:171], v2, s[44:45] offset:2048 sc1\n" \
"s_waitcnt vmcnt(32)\n" \
"v_mfma_f32_16x16x32_bf16 v[8:11], a[44:47], v[172:175], v[8:11]\n" \
"v_mfma_f32_16x16x32_bf16 v[12:15], a[172:175], v[172:175], v[12:15]\n" \
"global_load_dwordx4 v[172:175], v2, s[44:45] offset:3072 sc1\n" \
"s_waitcnt vmcnt(32)\n" \
"v_mfma_f32_16x16x32_bf16 v[8:11], a[48:51], v[176:179], v[8:11]\n" \
"v_mfma_f32_16x16x32_bf16 v[12:15], a[176:179], v[176:179], v[12:15]\n" \
"global_load_dwordx4 v[176:179], v3, s[44:45] offset:0 sc1\n" \
"s_waitcnt vmcnt(32)\n" \
"v_mfma_f32_16x16x32_bf16 v[8:11], a[52:55], v[180:183], v[8:11]\n" \
"v_mfma_f32_16x16x32_bf16 v[12:15], a[180:183], v[180:183], v[12:15]\n" \
"global_load_dwordx4 v[180:183], v3, s[44:45] offset:1024 sc1\n" \
"s_waitcnt vmcnt(32)\n" \
"v_mfma_f32_16x16x32_bf16 v[8:11], a[56:59], v[184:187], v[8:11]\n" \
"v_mfma_f32_16x16x32_bf16 v[12:15], a[184:187], v[184:187], v[12:15]\n" \
"global_load_dwordx4 v[184:187], v3, s[44:45] offset:2048 sc1\n" \
"s_waitcnt vmcnt(32)\n" \
"v_mfma_f32_16x16x32_bf16 v[8:11], a[60:63], v[188:191], v[8:11]\n" \
"v_mfma_f32_16x16x32_bf16 v[12:15], a[188:191], v[188:191], v[12:15]\n" \
"global_load_dwordx4 v[188:191], v3, s[44:45] offset:3072 sc1\n" \
"s_waitcnt vmcnt(32)\n" \
"v_mfma_f32_16x16x32_bf16 v[8:11], a[64:67], v[192:195], v[8:11]\n" \
"v_mfma_f32_16x16x32_bf16 v[12:15], a[192:195], v[192:195], v[12:15]\n" \
"global_load_dwordx4 v[192:195], v4, s[44:45] offset:0 sc1\n" \
"s_waitcnt vmcnt(32)\n" \
"v_mfma_f32_16x16x32_bf16 v[8:11], a[68:71], v[196:199], v[8:11]\n" \
"v_mfma_f32_16x16x32_bf16 v[12:15], a[196:199], v[196:199], v[12:15]\n" \
"global_load_dwordx4 v[196:199], v4, s[44:45] offset:1024 sc1\n" \
"s_waitcnt vmcnt(32)\n" \
"v_mfma_f32_16x16x32_bf16 v[8:11], a[72:75], v[200:203], v[8:11]\n" \
"v_mfma_f32_16x16x32_bf16 v[12:15], a[200:203], v[200:203], v[12:15]\n" \
"global_load_dwordx4 v[200:203], v4, s[44:45] offset:2048 sc1\n" \
"s_waitcnt vmcnt(32)\n" \
"v_mfma_f32_16x16x32_bf16 v[8:11], a[76:79], v[204:207], v[8:11]\n" \
"v_mfma_f32_16x16x32_bf16 v[12:15], a[204:207], v[204:207], v[12:15]\n" \
"global_load_dwordx4 v[204:207], v4, s[44:45] offset:3072 sc1\n" \
"s_waitcnt vmcnt(32)\n" \
"v_mfma_f32_16x16x32_bf16 v[8:11], a[80:83], v[208:211], v[8:11]\n" \
"v_mfma_f32_16x16x32_bf16 v[12:15], a[208:211], v[208:211], v[12:15]\n" \
"global_load_dwordx4 v[208:211], v5, s[44:45] offset:0 sc1\n" \
"s_waitcnt vmcnt(32)\n" \
"v_mfma_f32_16x16x32_bf16 v[8:11], a[84:87], v[212:215], v[8:11]\n" \
"v_mfma_f32_16x16x32_bf16 v[12:15], a[212:215], v[212:215], v[12:15]\n" \
"global_load_dwordx4 v[212:215], v5, s[44:45] offset:1024 sc1\n" \
"s_waitcnt vmcnt(32)\n" \
"v_mfma_f32_16x16x32_bf16 v[8:11], a[88:91], v[216:219], v[8:11]\n" \
"v_mfma_f32_16x16x32_bf16 v[12:15], a[216:219], v[216:219], v[12:15]\n" \
"global_load_dwordx4 v[216:219], v5, s[44:45] offset:2048 sc1\n" \
"s_waitcnt vmcnt(32)\n" \
"v_mfma_f32_16x16x32_bf16 v[8:11], a[92:95], v[220:223], v[8:11]\n" \
"v_mfma_f32_16x16x32_bf16 v[12:15], a[220:223], v[220:223], v[12:15]\n" \
"global_load_dwordx4 v[220:223], v5, s[44:45] offset:3072 sc1\n" \
"s_waitcnt vmcnt(32)\n" \
"v_mfma_f32_16x16x32_bf16 v[8:11], a[96:99], v[224:227], v[8:11]\n" \
"v_mfma_f32_16x16x32_bf16 v[12:15], a[224:227], v[224:227], v[12:15]\n" \
"global_load_dwordx4 v[224:227], v6, s[44:45] offset:0 sc1\n" \
"s_waitcnt vmcnt(32)\n" \
"v_mfma_f32_16x16x32_bf16 v[8:11], a[100:103], v[228:231], v[8:11]\n" \
"v_mfma_f32_16x16x32_bf16 v[12:15], a[228:231], v[228:231], v[12:15]\n" \
"global_load_dwordx4 v[228:231], v6, s[44:45] offset:1024 sc1\n" \
"s_waitcnt vmcnt(32)\n" \
"v_mfma_f32_16x16x32_bf16 v[8:11], a[104:107], v[232:235], v[8:11]\n" \
"v_mfma_f32_16x16x32_bf16 v[12:15], a[232:235], v[232:235], v[12:15]\n" \
"global_load_dwordx4 v[232:235], v6, s[44:45] offset:2048 sc1\n" \
"s_waitcnt vmcnt(32)\n" \
"v_mfma_f32_16x16x32_bf16 v[8:11], a[108:111], v[236:239], v[8:11]\n" \
"v_mfma_f32_16x16x32_bf16 v[12:15], a[236:239], v[236:239], v[12:15]\n" \
"global_load_dwordx4 v[236:239], v6, s[44:45] offset:3072 sc1\n" \
"s_waitcnt vmcnt(32)\n" \
"v_mfma_f32_16x16x32_bf16 v[8:11], a[112:115], v[240:243], v[8:11]\n" \
"v_mfma_f32_16x16x32_bf16 v[12:15], a[240:243], v[240:243], v[12:15]\n" \
"global_load_dwordx4 v[240:243], v7, s[44:45] offset:0 sc1\n" \
"s_waitcnt vmcnt(32)\n" \
"v_mfma_f32_16x16x32_bf16 v[8:11], a[116:119], v[244:247], v[8:11]\n" \
"v_mfma_f32_16x16x32_bf16 v[12:15], a[244:247], v[244:247], v[12:15]\n" \
"global_load_dwordx4 v[244:247], v7, s[44:45] offset:1024 sc1\n" \
"s_waitcnt vmcnt(32)\n" \
"v_mfma_f32_16x16x32_bf16 v[8:11], a[120:123], v[248:251], v[8:11]\n" \
"v_mfma_f32_16x16x32_bf16 v[12:15], a[248:251], v[248:251], v[12:15]\n" \
"global_load_dwordx4 v[248:251], v7, s[44:45] offset:2048 sc1\n" \
"s_waitcnt vmcnt(32)\n" \
"v_mfma_f32_16x16x32_bf16 v[8:11], a[124:127], v[252:255], v[8:11]\n" \
"v_mfma_f32_16x16x32_bf16 v[12:15], a[252:255], v[252:255], v[12:15]\n" \
"global_load_dwordx4 v[252:255], v7, s[44:45] offset:3072 sc1\n" \
"s_waitcnt vmcnt(32)\n" \
"s_nop 1\n" \
"v_min_u32_dpp v17, v17, v17 row_ror:4 row_mask:0xf bank_mask:0xf\n" \
"s_nop 1\n" \
"v_min_u32_dpp v17, v17, v17 row_ror:2 row_mask:0xf bank_mask:0xf\n" \
"s_nop 1\n" \
"v_min_u32_dpp v17, v17, v17 row_ror:1 row_mask:0xf bank_mask:0xf\n" \
"s_nop 1\n" \
"v_readfirstlane_b32 s51, v17\n" \
"s_mov_b32 s57, 1\n" \
"s_branch Lu_red%=\n" \
"Lu_drain%=:\n" \
"s_cmp_eq_u32 s50, 63\n" \
"s_cbranch_scc1 Lu_last%=\n" \
"s_waitcnt vmcnt(31)\n" \
"v_mfma_f32_16x16x32_bf16 v[8:11], a[0:3], v[128:131], 0\n" \
"v_mfma_f32_16x16x32_bf16 v[12:15], a[128:131], v[128:131], 0\n" \
"s_waitcnt vmcnt(30)\n" \
"v_mfma_f32_16x16x32_bf16 v[8:11], a[4:7], v[132:135], v[8:11]\n" \
"v_mfma_f32_16x16x32_bf16 v[12:15], a[132:135], v[132:135], v[12:15]\n" \
"s_waitcnt vmcnt(29)\n" \
"v_mfma_f32_16x16x32_bf16 v[8:11], a[8:11], v[136:139], v[8:11]\n" \
"v_mfma_f32_16x16x32_bf16 v[12:15], a[136:139], v[136:139], v[12:15]\n" \
"s_waitcnt vmcnt(28)\n" \
"v_mfma_f32_16x16x32_bf16 v[8:11], a[12:15], v[140:143], v[8:11]\n" \
"v_mfma_f32_16x16x32_bf16 v[12:15], a[140:143], v[140:143], v[12:15]\n" \
"s_waitcnt vmcnt(27)\n" \
"v_mfma_f32_16x16x32_bf16 v[8:11], a[16:19], v[144:147], v[8:11]\n" \
"v_mfma_f32_16x16x32_bf16 v[12:15], a[144:147], v[144:147], v[12:15]\n" \
"s_waitcnt vmcnt(26)\n" \
"v_mfma_f32_16x16x32_bf16 v[8:11], a[20:23], v[148:151], v[8:11]\n" \
"v_mfma_f32_16x16x32_bf16 v[12:15], a[148:151], v[148:151], v[12:15]\n" \
"s_waitcnt vmcnt(25)\n" \
"v_mfma_f32_16x16x32_bf16 v[8:11], a[24:27], v[152:155], v[8:11]\n" \
"v_mfma_f32_16x16x32_bf16 v[12:15], a[152:155], v[152:155], v[12:15]\n" \
"s_waitcnt vmcnt(24)\n" \
"v_mfma_f32_16x16x32_bf16 v[8:11], a[28:31], v[156:159], v[8:11]\n" \
"v_mfma_f32_16x16x32_bf16 v[12:15], a[156:159], v[156:159], v[12:15]\n" \
"s_waitcnt vmcnt(23)\n" \
"v_mfma_f32_16x16x32_bf16 v[8:11], a[32:35], v[160:163], v[8:11]\n" \
"v_mfma_f32_16x16x32_bf16 v[12:15], a[160:163], v[160:163], v[12:15]\n" \
"s_waitcnt vmcnt(22)\n" \
"v_mfma_f32_16x16x32_bf16 v[8:11], a[36:39], v[164:167], v[8:11]\n" \
"v_mfma_f32_16x16x32_bf16 v[12:15], a[164:167], v[164:167], v[12:15]\n" \
"s_waitcnt vmcnt(21)\n" \
"v_mfma_f32_16x16x32_bf16 v[8:11], a[40:43], v[168:171], v[8:11]\n" \
"v_mfma_f32_16x16x32_bf16 v[12:15], a[168:171], v[168:171], v[12:15]\n" \
"s_waitcnt vmcnt(20)\n" \
"v_mfma_f32_16x16x32_bf16 v[8:11], a[44:47], v[172:175], v[8:11]\n" \
"v_mfma_f32_16x16x32_bf16 v[12:15], a[172:175], v[172:175], v[12:15]\n" \
"s_waitcnt vmcnt(19)\n" \
"v_mfma_f32_16x16x32_bf16 v[8:11], a[48:51], v[176:179], v[8:11]\n" \
"v_mfma_f32_16x16x32_bf16 v[12:15], a[176:179], v[176:179], v[12:15]\n" \
"s_waitcnt vmcnt(18)\n" \
"v_mfma_f32_16x16x32_bf16 v[8:11], a[52:55], v[180:183], v[8:11]\n" \
"v_mfma_f32_16x16x32_bf16 v[12:15], a[180:183], v[180:183], v[12:15]\n" \
"s_waitcnt vmcnt(17)\n" \
"v_mfma_f32_16x16x32_bf16 v[8:11], a[56:59], v[184:187], v[8:11]\n" \
"v_mfma_f32_16x16x32_bf16 v[12:15], a[184:187], v[184:187], v[12:15]\n" \
"s_waitcnt vmcnt(16)\n" \
"v_mfma_f32_16x16x32_bf16 v[8:11], a[60:63], v[188:191], v[8:11]\n" \
"v_mfma_f32_16x16x32_bf16 v[12:15], a[188:191], v[188:191], v[12:15]\n" \
"s_waitcnt vmcnt(15)\n" \
"v_mfma_f32_16x16x32_bf16 v[8:11], a[64:67], v[192:195], v[8:11]\n" \
"v_mfma_f32_16x16x32_bf16 v[12:15], a[192:195], v[192:195], v[12:15]\n" \
"s_waitcnt vmcnt(14)\n" \
"v_mfma_f32_16x16x32_bf16 v[8:11], a[68:71], v[196:199], v[8:11]\n" \
"v_mfma_f32_16x16x32_bf16 v[12:15], a[196:199], v[196:199], v[12:15]\n" \
"s_waitcnt vmcnt(13)\n" \
"v_mfma_f32_16x16x32_bf16 v[8:11], a[72:75], v[200:203], v[8:11]\n" \
"v_mfma_f32_16x16x32_bf16 v[12:15], a[200:203], v[200:203], v[12:15]\n" \
"s_waitcnt vmcnt(12)\n" \
"v_mfma_f32_16x16x32_bf16 v[8:11], a[76:79], v[204:207], v[8:11]\n" \
"v_mfma_f32_16x16x32_bf16 v[12:15], a[204:207], v[204:207], v[12:15]\n" \
"s_waitcnt vmcnt(11)\n" \
"v_mfma_f32_16x16x32_bf16 v[8:11], a[80:83], v[208:211], v[8:11]\n" \
"v_mfma_f32_16x16x32_bf16 v[12:15], a[208:211], v[208:211], v[12:15]\n" \
"s_waitcnt vmcnt(10)\n" \
"v_mfma_f32_16x16x32_bf16 v[8:11], a[84:87], v[212:215], v[8:11]\n" \
"v_mfma_f32_16x16x32_bf16 v[12:15], a[212:215], v[212:215], v[12:15]\n" \
"s_waitcnt vmcnt(9)\n" \
"v_mfma_f32_16x16x32_bf16 v[8:11], a[88:91], v[216:219], v[8:11]\n" \
"v_mfma_f32_16x16x32_bf16 v[12:15], a[216:219], v[216:219], v[12:15]\n" \
"s_waitcnt vmcnt(8)\n" \
"v_mfma_f32_16x16x32_bf16 v[8:11], a[92:95], v[220:223], v[8:11]\n" \
"v_mfma_f32_16x16x32_bf16 v[12:15], a[220:223], v[220:223], v[12:15]\n" \
"s_waitcnt vmcnt(7)\n" \
"v_mfma_f32_16x16x32_bf16 v[8:11], a[96:99], v[224:227], v[8:11]\n" \
"v_mfma_f32_16x16x32_bf16 v[12:15], a[224:227], v[224:227], v[12:15]\n" \
"s_waitcnt vmcnt(6)\n" \
"v_mfma_f32_16x16x32_bf16 v[8:11], a[100:103], v[228:231], v[8:11]\n" \
"v_mfma_f32_16x16x32_bf16 v[12:15], a[228:231], v[228:231], v[12:15]\n" \
"s_waitcnt vmcnt(5)\n" \
"v_mfma_f32_16x16x32_bf16 v[8:11], a[104:107], v[232:235], v[8:11]\n" \
"v_mfma_f32_16x16x32_bf16 v[12:15], a[232:235], v[232:235], v[12:15]\n" \
"s_waitcnt vmcnt(4)\n" \
"v_mfma_f32_16x16x32_bf16 v[8:11], a[108:111], v[236:239], v[8:11]\n" \
"v_mfma_f32_16x16x32_bf16 v[12:15], a[236:239], v[236:239], v[12:15]\n" \
"s_waitcnt vmcnt(3)\n" \
"v_mfma_f32_16x16x32_bf16 v[8:11], a[112:115], v[240:243], v[8:11]\n" \
"v_mfma_f32_16x16x32_bf16 v[12:15], a[240:243], v[240:243], v[12:15]\n" \
"s_waitcnt vmcnt(2)\n" \
"v_mfma_f32_16x16x32_bf16 v[8:11], a[116:119], v[244:247], v[8:11]\n" \
"v_mfma_f32_16x16x32_bf16 v[12:15], a[244:247], v[244:247], v[12:15]\n" \
"s_waitcnt vmcnt(1)\n" \
"v_mfma_f32_16x16x32_bf16 v[8:11], a[120:123], v[248:251], v[8:11]\n" \
"v_mfma_f32_16x16x32_bf16 v[12:15], a[248:251], v[248:251], v[12:15]\n" \
"s_waitcnt vmcnt(0)\n" \
"v_mfma_f32_16x16x32_bf16 v[8:11], a[124:127], v[252:255], v[8:11]\n" \
"v_mfma_f32_16x16x32_bf16 v[12:15], a[252:255], v[252:255], v[12:15]\n" \
"s_mov_b32 s57, 0\n" \
"Lu_red%=:\n" \
"s_and_b32 s55, s53, 1\n" \
"s_lshl_b32 s55, s55, 13\n" \
"v_add_u32 v22, s55, v19\n" \
"v_add_u32 v23, s55, v20\n" \
"s_and_b32 s55, s53, 3\n" \
"s_nop 4\n" \
"ds_write_b128 v22, v[8:11]\n" \
"ds_write_b128 v22, v[12:15] offset:1024\n" \
"s_cmp_eq_u32 s55, s52\n" \
"s_waitcnt lgkmcnt(0)\n" \
"s_barrier\n" \
"s_cbranch_scc0 Lu_nored%=\n" \
"ds_read_b128 v[24:27], v23 offset:0\n" \
"ds_read_b128 v[40:43], v23 offset:1024\n" \
"ds_read_b128 v[28:31], v23 offset:2048\n" \
"ds_read_b128 v[44:47], v23 offset:3072\n" \
"ds_read_b128 v[32:35], v23 offset:4096\n" \
"ds_read_b128 v[48:51], v23 offset:5120\n" \
"ds_read_b128 v[36:39], v23 offset:6144\n" \
"ds_read_b128 v[52:55], v23 offset:7168\n" \
"s_waitcnt lgkmcnt(0)\n" \
"v_pk_add_f32 v[24:25], v[24:25], v[28:29]\n" \
"v_pk_add_f32 v[26:27], v[26:27], v[30:31]\n" \
"v_pk_add_f32 v[40:41], v[40:41], v[44:45]\n" \
"v_pk_add_f32 v[42:43], v[42:43], v[46:47]\n" \
"v_pk_add_f32 v[24:25], v[24:25], v[32:33]\n" \
"v_pk_add_f32 v[26:27], v[26:27], v[34:35]\n" \
"v_pk_add_f32 v[40:41], v[40:41], v[48:49]\n" \
"v_pk_add_f32 v[42:43], v[42:43], v[50:51]\n" \
"v_pk_add_f32 v[24:25], v[24:25], v[36:37]\n" \
"v_pk_add_f32 v[26:27], v[26:27], v[38:39]\n" \
"v_pk_add_f32 v[40:41], v[40:41], v[52:53]\n" \
"v_pk_add_f32 v[42:43], v[42:43], v[54:55]\n" \
"v_cvt_pk_bf16_f32 v56, v24, v25\n" \
"v_cvt_pk_bf16_f32 v57, v26, v27\n" \
"v_cvt_pk_bf16_f32 v58, v40, v41\n" \
"v_cvt_pk_bf16_f32 v59, v42, v43\n" \
"global_store_dwordx4 v18, v[56:59], s[46:47] nt\n" \
"Lu_nored%=:\n" \
"s_add_u32 s50, s50, 2\n" \
"s_add_u32 s53, s53, 1\n" \
"s_add_u32 s46, s46, 0x28000\n" \
"s_addc_u32 s47, s47, 0\n" \
"s_mov_b64 s[42:43], s[44:45]\n" \
"s_add_u32 s44, s44, 0x80000\n" \
"s_addc_u32 s45, s45, 0\n" \
"s_cmp_ge_u32 s50, 64\n" \
"s_cbranch_scc1 Lu_done%=\n" \
"s_cmp_eq_u32 s57, 0\n" \
"s_cbranch_scc1 Lu_fresh%=\n" \
"s_branch Lu_top%=\n" \
"Lu_last%=:\n" \
"s_mov_b32 s54, 0\n" \
"Lu_last2%=:\n" \
"s_waitcnt vmcnt(31)\n" \
"v_mfma_f32_16x16x32_bf16 v[8:11], a[0:3], v[128:131], 0\n" \
"v_mfma_f32_16x16x32_bf16 v[12:15], a[128:131], v[128:131], 0\n" \
"v_max3_u32 v21, v128, v129, v130\n" \
"v_max_u32 v21, v21, v131\n" \
"s_waitcnt vmcnt(30)\n" \
"v_mfma_f32_16x16x32_bf16 v[8:11], a[4:7], v[132:135], v[8:11]\n" \
"v_mfma_f32_16x16x32_bf16 v[12:15], a[132:135], v[132:135], v[12:15]\n" \
"v_max3_u32 v21, v21, v132, v133\n" \
"v_max3_u32 v21, v21, v134, v135\n" \
"s_waitcnt vmcnt(29)\n" \
"v_mfma_f32_16x16x32_bf16 v[8:11], a[8:11], v[136:139], v[8:11]\n" \
"v_mfma_f32_16x16x32_bf16 v[12:15], a[136:139], v[136:139], v[12:15]\n" \
"v_max3_u32 v21, v21, v136, v137\n" \
"v_max3_u32 v21, v21, v138, v139\n" \
"s_waitcnt vmcnt(28)\n" \
"v_mfma_f32_16x16x32_bf16 v[8:11], a[12:15], v[140:143], v[8:11]\n" \
"v_mfma_f32_16x16x32_bf16 v[12:15], a[140:143], v[140:143], v[12:15]\n" \
"v_max3_u32 v21, v21, v140, v141\n" \
"v_max3_u32 v21, v21, v142, v143\n" \
"s_waitcnt vmcnt(27)\n" \
"v_mfma_f32_16x16x32_bf16 v[8:11], a[16:19], v[144:147], v[8:11]\n" \
"v_mfma_f32_16x16x32_bf16 v[12:15], a[144:147], v[144:147], v[12:15]\n" \
"v_max3_u32 v21, v21, v144, v145\n" \
"v_max3_u32 v21, v21, v146, v147\n" \
"s_waitcnt vmcnt(26)\n" \
"v_mfma_f32_16x16x32_bf16 v[8:11], a[20:23], v[148:151], v[8:11]\n" \
"v_mfma_f32_16x16x32_bf16 v[12:15], a[148:151], v[148:151], v[12:15]\n" \
"v_max3_u32 v21, v21, v148, v149\n" \
"v_max3_u32 v21, v21, v150, v151\n" \
"s_waitcnt vmcnt(25)\n" \
"v_mfma_f32_16x16x32_bf16 v[8:11], a[24:27], v[152:155], v[8:11]\n" \
"v_mfma_f32_16x16x32_bf16 v[12:15], a[152:155], v[152:155], v[12:15]\n" \
"v_max3_u32 v21, v21, v152, v153\n" \
"v_max3_u32 v21, v21, v154, v155\n" \
"s_waitcnt vmcnt(24)\n" \
"v_mfma_f32_16x16x32_bf16 v[8:11], a[28:31], v[156:159], v[8:11]\n" \
"v_mfma_f32_16x16x32_bf16 v[12:15], a[156:159], v[156:159], v[12:15]\n" \
"v_max3_u32 v21, v21, v156, v157\n" \
"v_max3_u32 v21, v21, v158, v159\n" \
"s_waitcnt vmcnt(23)\n" \
"v_mfma_f32_16x16x32_bf16 v[8:11], a[32:35], v[160:163], v[8:11]\n" \
"v_mfma_f32_16x16x32_bf16 v[12:15], a[160:163], v[160:163], v[12:15]\n" \
"v_max3_u32 v21, v21, v160, v161\n" \
"v_max3_u32 v21, v21, v162, v163\n" \
"s_waitcnt vmcnt(22)\n" \
"v_mfma_f32_16x16x32_bf16 v[8:11], a[36:39], v[164:167], v[8:11]\n" \
"v_mfma_f32_16x16x32_bf16 v[12:15], a[164:167], v[164:167], v[12:15]\n" \
"v_max3_u32 v21, v21, v164, v165\n" \
"v_max3_u32 v21, v21, v166, v167\n" \
"s_waitcnt vmcnt(21)\n" \
"v_mfma_f32_16x16x32_bf16 v[8:11], a[40:43], v[168:171], v[8:11]\n" \
"v_mfma_f32_16x16x32_bf16 v[12:15], a[168:171], v[168:171], v[12:15]\n" \
"v_max3_u32 v21, v21, v168, v169\n" \
"v_max3_u32 v21, v21, v170, v171\n" \
"s_waitcnt vmcnt(20)\n" \
"v_mfma_f32_16x16x32_bf16 v[8:11], a[44:47], v[172:175], v[8:11]\n" \
"v_mfma_f32_16x16x32_bf16 v[12:15], a[172:175], v[172:175], v[12:15]\n" \
"v_max3_u32 v21, v21, v172, v173\n" \
"v_max3_u32 v21, v21, v174, v175\n" \
"s_waitcnt vmcnt(19)\n" \
"v_mfma_f32_16x16x32_bf16 v[8:11], a[48:51], v[176:179], v[8:11]\n" \
"v_mfma_f32_16x16x32_bf16 v[12:15], a[176:179], v[176:179], v[12:15]\n" \
"v_max3_u32 v21, v21, v176, v177\n" \
"v_max3_u32 v21, v21, v178, v179\n" \
"s_waitcnt vmcnt(18)\n" \
"v_mfma_f32_16x16x32_bf16 v[8:11], a[52:55], v[180:183], v[8:11]\n" \
"v_mfma_f32_16x16x32_bf16 v[12:15], a[180:183], v[180:183], v[12:15]\n" \
"v_max3_u32 v21, v21, v180, v181\n" \
"v_max3_u32 v21, v21, v182, v183\n" \
"s_waitcnt vmcnt(17)\n" \
"v_mfma_f32_16x16x32_bf16 v[8:11], a[56:59], v[184:187], v[8:11]\n" \
"v_mfma_f32_16x16x32_bf16 v[12:15], a[184:187], v[184:187], v[12:15]\n" \
"v_max3_u32 v21, v21, v184, v185\n" \
"v_max3_u32 v21, v21, v186, v187\n" \
"s_waitcnt vmcnt(16)\n" \
"v_mfma_f32_16x16x32_bf16 v[8:11], a[60:63], v[188:191], v[8:11]\n" \
"v_mfma_f32_16x16x32_bf16 v[12:15], a[188:191], v[188:191], v[12:15]\n" \
"v_max3_u32 v21, v21, v188, v189\n" \
"v_max3_u32 v21, v21, v190, v191\n" \
"s_waitcnt vmcnt(15)\n" \
"v_mfma_f32_16x16x32_bf16 v[8:11], a[64:67], v[192:195], v[8:11]\n" \
"v_mfma_f32_16x16x32_bf16 v[12:15], a[192:195], v[192:195], v[12:15]\n" \
"v_max3_u32 v21, v21, v192, v193\n" \
"v_max3_u32 v21, v21, v194, v195\n" \
"s_waitcnt vmcnt(14)\n" \
"v_mfma_f32_16x16x32_bf16 v[8:11], a[68:71], v[196:199], v[8:11]\n" \
"v_mfma_f32_16x16x32_bf16 v[12:15], a[196:199], v[196:199], v[12:15]\n" \
"v_max3_u32 v21, v21, v196, v197\n" \
"v_max3_u32 v21, v21, v198, v199\n" \
"s_waitcnt vmcnt(13)\n" \
"v_mfma_f32_16x16x32_bf16 v[8:11], a[72:75], v[200:203], v[8:11]\n" \
"v_mfma_f32_16x16x32_bf16 v[12:15], a[200:203], v[200:203], v[12:15]\n" \
"v_max3_u32 v21, v21, v200, v201\n" \
"v_max3_u32 v21, v21, v202, v203\n" \
"s_waitcnt vmcnt(12)\n" \
"v_mfma_f32_16x16x32_bf16 v[8:11], a[76:79], v[204:207], v[8:11]\n" \
"v_mfma_f32_16x16x32_bf16 v[12:15], a[204:207], v[204:207], v[12:15]\n" \
"v_max3_u32 v21, v21, v204, v205\n" \
"v_max3_u32 v21, v21, v206, v207\n" \
"s_waitcnt vmcnt(11)\n" \
"v_mfma_f32_16x16x32_bf16 v[8:11], a[80:83], v[208:211], v[8:11]\n" \
"v_mfma_f32_16x16x32_bf16 v[12:15], a[208:211], v[208:211], v[12:15]\n" \
"v_max3_u32 v21, v21, v208, v209\n" \
"v_max3_u32 v21, v21, v210, v211\n" \
"s_waitcnt vmcnt(10)\n" \
"v_mfma_f32_16x16x32_bf16 v[8:11], a[84:87], v[212:215], v[8:11]\n" \
"v_mfma_f32_16x16x32_bf16 v[12:15], a[212:215], v[212:215], v[12:15]\n" \
"v_max3_u32 v21, v21, v212, v213\n" \
"v_max3_u32 v21, v21, v214, v215\n" \
"s_waitcnt vmcnt(9)\n" \
"v_mfma_f32_16x16x32_bf16 v[8:11], a[88:91], v[216:219], v[8:11]\n" \
"v_mfma_f32_16x16x32_bf16 v[12:15], a[216:219], v[216:219], v[12:15]\n" \
"v_max3_u32 v21, v21, v216, v217\n" \
"v_max3_u32 v21, v21, v218, v219\n" \
"s_waitcnt vmcnt(8)\n" \
"v_mfma_f32_16x16x32_bf16 v[8:11], a[92:95], v[220:223], v[8:11]\n" \
"v_mfma_f32_16x16x32_bf16 v[12:15], a[220:223], v[220:223], v[12:15]\n" \
"v_max3_u32 v21, v21, v220, v221\n" \
"v_max3_u32 v21, v21, v222, v223\n" \
"s_waitcnt vmcnt(7)\n" \
"v_mfma_f32_16x16x32_bf16 v[8:11], a[96:99], v[224:227], v[8:11]\n" \
"v_mfma_f32_16x16x32_bf16 v[12:15], a[224:227], v[224:227], v[12:15]\n" \
"v_max3_u32 v21, v21, v224, v225\n" \
"v_max3_u32 v21, v21, v226, v227\n" \
"s_waitcnt vmcnt(6)\n" \
"v_mfma_f32_16x16x32_bf16 v[8:11], a[100:103], v[228:231], v[8:11]\n" \
"v_mfma_f32_16x16x32_bf16 v[12:15], a[228:231], v[228:231], v[12:15]\n" \
"v_max3_u32 v21, v21, v228, v229\n" \
"v_max3_u32 v21, v21, v230, v231\n" \
"s_waitcnt vmcnt(5)\n" \
"v_mfma_f32_16x16x32_bf16 v[8:11], a[104:107], v[232:235], v[8:11]\n" \
"v_mfma_f32_16x16x32_bf16 v[12:15], a[232:235], v[232:235], v[12:15]\n" \
"v_max3_u32 v21, v21, v232, v233\n" \
"v_max3_u32 v21, v21, v234, v235\n" \
"s_waitcnt vmcnt(4)\n" \
"v_mfma_f32_16x16x32_bf16 v[8:11], a[108:111], v[236:239], v[8:11]\n" \
"v_mfma_f32_16x16x32_bf16 v[12:15], a[236:239], v[236:239], v[12:15]\n" \
"v_max3_u32 v21, v21, v236, v237\n" \
"v_max3_u32 v21, v21, v238, v239\n" \
"s_waitcnt vmcnt(3)\n" \
"v_mfma_f32_16x16x32_bf16 v[8:11], a[112:115], v[240:243], v[8:11]\n" \
"v_mfma_f32_16x16x32_bf16 v[12:15], a[240:243], v[240:243], v[12:15]\n" \
"v_max3_u32 v21, v21, v240, v241\n" \
"v_max3_u32 v21, v21, v242, v243\n" \
"s_waitcnt vmcnt(2)\n" \
"v_mfma_f32_16x16x32_bf16 v[8:11], a[116:119], v[244:247], v[8:11]\n" \
"v_mfma_f32_16x16x32_bf16 v[12:15], a[244:247], v[244:247], v[12:15]\n" \
"v_max3_u32 v21, v21, v244, v245\n" \
"v_max3_u32 v21, v21, v246, v247\n" \
"s_waitcnt vmcnt(1)\n" \
"v_mfma_f32_16x16x32_bf16 v[8:11], a[120:123], v[248:251], v[8:11]\n" \
"v_mfma_f32_16x16x32_bf16 v[12:15], a[248:251], v[248:251], v[12:15]\n" \
"v_max3_u32 v21, v21, v248, v249\n" \
"v_max3_u32 v21, v21, v250, v251\n" \
"s_waitcnt vmcnt(0)\n" \
"v_mfma_f32_16x16x32_bf16 v[8:11], a[124:127], v[252:255], v[8:11]\n" \
"v_mfma_f32_16x16x32_bf16 v[12:15], a[252:255], v[252:255], v[12:15]\n" \
"v_max3_u32 v21, v21, v252, v253\n" \
"v_max3_u32 v21, v21, v254, v255\n" \
"v_cmp_eq_u32 vcc, -1, v21\n" \
"s_mov_b32 s57, 0\n" \
"s_cbranch_vccz Lu_red%=\n" \
"s_add_u32 s54, s54, 1\n" \
"s_cmp_lt_u32 s54, 262144\n" \
"s_cbranch_scc0 Lu_fail%=\n" \
"global_load_dwordx4 v[128:131], v0, s[42:43] offset:0 sc1\n" \
"global_load_dwordx4 v[132:135], v0, s[42:43] offset:1024 sc1\n" \
"global_load_dwordx4 v[136:139], v0, s[42:43] offset:2048 sc1\n" \
"global_load_dwordx4 v[140:143], v0, s[42:43] offset:3072 sc1\n" \
"global_load_dwordx4 v[144:147], v1, s[42:43] offset:0 sc1\n" \
"global_load_dwordx4 v[148:151], v1, s[42:43] offset:1024 sc1\n" \
"global_load_dwordx4 v[152:155], v1, s[42:43] offset:2048 sc1\n" \
"global_load_dwordx4 v[156:159], v1, s[42:43] offset:3072 sc1\n" \
"global_load_dwordx4 v[160:163], v2, s[42:43] offset:0 sc1\n" \
"global_load_dwordx4 v[164:167], v2, s[42:43] offset:1024 sc1\n" \
"global_load_dwordx4 v[168:171], v2, s[42:43] offset:2048 sc1\n" \
"global_load_dwordx4 v[172:175], v2, s[42:43] offset:3072 sc1\n" \
"global_load_dwordx4 v[176:179], v3, s[42:43] offset:0 sc1\n" \
"global_load_dwordx4 v[180:183], v3, s[42:43] offset:1024 sc1\n" \
"global_load_dwordx4 v[184:187], v3, s[42:43] offset:2048 sc1\n" \
"global_load_dwordx4 v[188:191], v3, s[42:43] offset:3072 sc1\n" \
"global_load_dwordx4 v[192:195], v4, s[42:43] offset:0 sc1\n" \
"global_load_dwordx4 v[196:199], v4, s[42:43] offset:1024 sc1\n" \
"global_load_dwordx4 v[200:203], v4, s[42:43] offset:2048 sc1\n" \
"global_load_dwordx4 v[204:207], v4, s[42:43] offset:3072 sc1\n" \
"global_load_dwordx4 v[208:211], v5, s[42:43] offset:0 sc1\n" \
"global_load_dwordx4 v[212:215], v5, s[42:43] offset:1024 sc1\n" \
"global_load_dwordx4 v[216:219], v5, s[42:43] offset:2048 sc1\n" \
"global_load_dwordx4 v[220:223], v5, s[42:43] offset:3072 sc1\n" \
"global_load_dwordx4 v[224:227], v6, s[42:43] offset:0 sc1\n" \
"global_load_dwordx4 v[228:231], v6, s[42:43] offset:1024 sc1\n" \
"global_load_dwordx4 v[232:235], v6, s[42:43] offset:2048 sc1\n" \
"global_load_dwordx4 v[236:239], v6, s[42:43] offset:3072 sc1\n" \
"global_load_dwordx4 v[240:243], v7, s[42:43] offset:0 sc1\n" \
"global_load_dwordx4 v[244:247], v7, s[42:43] offset:1024 sc1\n" \
"global_load_dwordx4 v[248:251], v7, s[42:43] offset:2048 sc1\n" \
"global_load_dwordx4 v[252:255], v7, s[42:43] offset:3072 sc1\n" \
"s_branch Lu_last2%=\n" \
"Lu_fail%=:\n" \
"s_mov_b64 exec, 1\n" \
"v_mov_b32 v21, 16448\n" \
"v_mov_b32 v22, 5\n" \
"global_store_dword v21, v22, s[48:49] sc1\n" \
"s_mov_b64 exec, -1\n" \
"s_add_u32 s53, s50, 0\n" \
"Lu_fail2%=:\n" \
"s_cmp_ge_u32 s50, 64\n" \
"s_cbranch_scc1 Lu_done%=\n" \
"s_barrier\n" \
"s_add_u32 s50, s50, 2\n" \
"s_branch Lu_fail2%=\n" \
"Lu_done%=:\n" \
"s_endpgm\n" \
"Lk_end:\n" \
""
#define T3_CLOB "v255", "a255", "s40", "s41", "s42", "s43", "s44", "s45", "s46", "s47", "s48", "s49", "s50", "s51", "s52", "s53", "s54", "s55", "s56", "s57", "s58", "s59", "vcc", "scc", "memory"
#define P_W1 316211414
#define P_W2 1967143
#define P_W3 97
#define P_W4 126681088
#define P_W5 25362882
#define P_W6 3408132
#define D_A 0
#define A3_AUX 3
#define WS_END 50

#define NPROBE 5
#define NPROBE_MAX 16
#define D_T3 6
#define PREF 15
#define CLR_A 5632
#define PRB_N 5
#define PRB_D 6
#define D_CLR 0
#define T3_OUT 32
#define T3_MAP 0
#define T3_AUX 0
#define NT_W 0
#define NT_P 0
#define FL_KK 24
#define D_Q 3
namespace mk {
typedef __bf16 bf16;
typedef __bf16 bf16x8 __attribute__((ext_vector_type(8)));
typedef __bf16 bf16x4 __attribute__((ext_vector_type(4)));
typedef __bf16 bf16x2 __attribute__((ext_vector_type(2)));
typedef float f32x4 __attribute__((ext_vector_type(4)));
typedef float f32x16 __attribute__((ext_vector_type(16)));
typedef unsigned v4u __attribute__((ext_vector_type(4)));
typedef unsigned v2u __attribute__((ext_vector_type(2)));
#define LAS __attribute__((address_space(3)))
#define GAS __attribute__((address_space(1)))
typedef LAS char lchar;
typedef GAS unsigned gu32;
typedef __amdgpu_buffer_rsrc_t rsrc_t;

constexpr size_t PAGE = (size_t)2 << 20;
constexpr size_t FLAGS_OFF = 0x140000;
#define CODE_PF 1
#define A3_WB 0
#define D_AX 0
#define OX_TAGGED 1
constexpr int F_GO = 16384, F_ERR = 16448, F_PRB = 16512, F_A1 = 16640, F_A2 = 16768, F_WARM = 16896, F_EP = 17024, F_Z = 17408;
constexpr int F_T1 = 32768, F_OP = 34816, F_PROG = 36864, F_STAT = 49152, F_SIZE = 53248;
constexpr unsigned SENT = 0xFFFFFFFFu;
constexpr unsigned SPINMAX = 1u << 18;
constexpr int SC1 = 16;
constexpr unsigned OOB = 0x80000000u;
constexpr unsigned N_WARM = 32;

struct Args {
    const bf16* Wq; const bf16* Wk; const bf16* Wv; const bf16* Wo;
    char* comm; char* KX; char* VX; char* KM;
    char* VM; char* flags; unsigned* dbg; int w1; int w2;
    int w3; int w4; int w5; int w6;
    const bf16* Ax; const bf16* Awqkv; const bf16* Awo; const bf16* Bx;
    const bf16* mem0; char* out; char* QX; char* OX;
    char* QA; char* KA; char* VA; char* OA;
};

__device__ __forceinline__ unsigned xcc_id() { return (unsigned)__builtin_amdgcn_s_getreg(20 | (3 << 11)) & 0xfu; }
__device__ __forceinline__ unsigned long long clk() { unsigned long long t; asm volatile("s_memtime %0\n\ts_waitcnt lgkmcnt(0)" : "=s"(t) :: "memory"); return t; }
__device__ __forceinline__ unsigned pk(float a, float b) { bf16x2 v = {(__bf16)a, (__bf16)b}; return __builtin_bit_cast(unsigned, v); }
__device__ __forceinline__ rsrc_t mkrsrc(const void* p, unsigned bytes) { return __builtin_amdgcn_make_buffer_rsrc((void*)p, 0, bytes, 0x00020000); }
__device__ __forceinline__ v4u ld16(rsrc_t r, unsigned voff, unsigned soff) { return __builtin_amdgcn_raw_buffer_load_b128(r, voff, soff, SC1); }
__device__ __forceinline__ v2u ld8(rsrc_t r, unsigned voff, unsigned soff) { return __builtin_amdgcn_raw_buffer_load_b64(r, voff, soff, SC1); }
template <int AUX>
__device__ __forceinline__ void st16(v4u v, rsrc_t r, unsigned voff, unsigned soff) { __builtin_amdgcn_raw_buffer_store_b128(v, r, voff, soff, AUX); asm volatile("s_nop 1" :: "v"(v)); }
__device__ __forceinline__ bf16x8 asb(v4u v) { return __builtin_bit_cast(bf16x8, v); }
__device__ __forceinline__ f32x4 mma16(bf16x8 a, bf16x8 b, f32x4 c) { return __builtin_amdgcn_mfma_f32_16x16x32_bf16(a, b, c, 0, 0, 0); }
__device__ __forceinline__ f32x16 mma32(bf16x8 a, bf16x8 b, f32x16 c) { return __builtin_amdgcn_mfma_f32_32x32x16_bf16(a, b, c, 0, 0, 0); }
__device__ __forceinline__ bf16x4 trd(lchar* lds, unsigned off) { return __builtin_amdgcn_ds_read_tr16_b64_v4bf16((LAS bf16x4*)(lds + off)); }
__device__ __forceinline__ bf16x8 cat(bf16x4 a, bf16x4 b) { return __builtin_shufflevector(a, b, 0, 1, 2, 3, 4, 5, 6, 7); }
template <bool COH>
__device__ __forceinline__ void dma16(unsigned ldsaddr, unsigned voff, const void* sbase) {
    if (COH) asm volatile("s_mov_b32 m0, %0\n\ts_nop 0\n\tglobal_load_lds_dwordx4 %1, %2 sc1" :: "s"(ldsaddr), "v"(voff), "s"(sbase));
    else asm volatile("s_mov_b32 m0, %0\n\ts_nop 0\n\tglobal_load_lds_dwordx4 %1, %2" :: "s"(ldsaddr), "v"(voff), "s"(sbase));
}
template <bool NT>
__device__ __forceinline__ void dma16s(unsigned ldsaddr, unsigned voff, const void* sbase) {
    if (NT) asm volatile("s_mov_b32 m0, %0\n\ts_nop 0\n\tglobal_load_lds_dwordx4 %1, %2 nt" :: "s"(ldsaddr), "v"(voff), "s"(sbase));
    else dma16<false>(ldsaddr, voff, sbase);
}
#define WAITV(n) asm volatile("s_waitcnt vmcnt(" #n ")" ::: "memory")
#define BARV(n) asm volatile("s_waitcnt vmcnt(" #n ") lgkmcnt(0)\n\ts_barrier" ::: "memory")
#define BARL() asm volatile("s_waitcnt lgkmcnt(0)\n\ts_barrier" ::: "memory")
#define MARK(A, RID, I) do {} while (0)
#define MARKL(A, RID, I) do {} while (0)
__device__ __forceinline__ unsigned ldflag(const char* flags, unsigned off) { return __hip_atomic_load((gu32*)(flags + off), __ATOMIC_RELAXED, __HIP_MEMORY_SCOPE_AGENT); }
__device__ __forceinline__ void stflag(char* flags, unsigned off, unsigned v) { __hip_atomic_store((gu32*)(flags + off), v, __ATOMIC_RELAXED, __HIP_MEMORY_SCOPE_AGENT); }
__device__ __forceinline__ unsigned wait_ge(char* flags, unsigned off, unsigned need, int nap) {
    for (unsigned n = 0;; ++n) {
        unsigned v = ldflag(flags, off);
        if (__all(v >= need)) {
            for (int o = 1; o < 16; o <<= 1) v = min(v, (unsigned)__shfl_xor((int)v, o));
            return (unsigned)__builtin_amdgcn_readfirstlane((int)v);
        }
        if (n >= SPINMAX) { stflag(flags, F_ERR, 2); return 0xFFFFFFFFu; }
        for (int i = 0; i < nap; ++i) __builtin_amdgcn_s_sleep(2);
    }
}

__device__ __forceinline__ unsigned wait_go(char* flags, const unsigned tag);
__device__ __forceinline__ void arrive(const Args& a, unsigned off, int tid);

enum { M_KQ = 0, M_V = 1, M_AQK = 2, M_AV = 3, M_AO = 4 };
constexpr unsigned XBUF = 40960;
template <int MODE>
__device__ __forceinline__ void wgemm(const Args& a, lchar* lds, const char* W, const int ldw, const char* X, const int t0, const int tstep, const int n,
                                      char* dst, const unsigned dstbytes, const int unit, const unsigned progoff, const int lane, const int w, const int rid, const int mk0, const unsigned tag = 0) {
    const unsigned lb = (unsigned)(size_t)lds;
    if (progoff && lane < 2) stflag(a.flags, progoff + 4 * w + lane * 8192, 0);
    const int hi = lane >> 5, l31 = lane & 31;
    constexpr bool COH = (MODE == M_AO);
    bf16x8 Wf[40];
    unsigned voffX[10];
#pragma unroll
    for (int i = 0; i < 10; ++i) { const int G = 16 * i + 4 * w + (lane >> 4), row = G / 5, grp = G % 5; voffX[i] = row * 1280 + grp * 256 + (((lane & 15) ^ (row & 15)) << 4); }
    auto xdma = [&](int it, int i) { dma16<COH>(lb + ((it + 1) % 3) * XBUF + i * 4096 + w * 1024, voffX[i], X + (size_t)(t0 + it * tstep) * 40960); };
    {
        const unsigned voffW = (4 * w + (lane >> 4)) * ldw + (((lane & 15) ^ ((lane >> 4) << 2)) << 4);
        auto wdma = [&](int r) {
#pragma unroll
            for (int i = 0; i < 10; ++i) dma16s<((MODE <= M_V ? NT_W & 1 : NT_W & 2) != 0)>(lb + (r % 3) * XBUF + i * 4096 + w * 1024, voffW, W + (size_t)(160 * r + 16 * i) * ldw);
        };
        const int blk = (lane >> 4) & 1, q = (lane >> 2) & 3, p = lane & 3;
        const int ch = 4 * w + 2 * blk + (MODE == M_AO ? (p & 1) : (p >> 1)), sub = (MODE == M_AO ? (p >> 1) : (p & 1)) * 8;
        const unsigned wr = (8 * hi + q) * 256 + ((ch ^ (q << 2)) << 4) + sub;
        auto wread = [&](int r) {
#pragma unroll
            for (int k = 0; k < 10; ++k) { const unsigned o = (r % 3) * XBUF + wr + k * 4096; Wf[10 * r + k] = cat(trd(lds, o), trd(lds, o + 1024)); }
        };
        wdma(0); wdma(1); wdma(2);
        BARV(20);
        if (progoff && w == 0 && lane == 0) stflag(a.flags, F_Z + 4 * rid, tag);
        wread(0); BARL(); wdma(3);
        BARV(20); wread(1); BARL();
        if (MODE == M_AO) wait_ge(a.flags, F_A2, 32, 4);
#pragma unroll
        for (int i = 0; i < 10; ++i) xdma(0, i);
        BARV(20); wread(2); BARL();
        if (n > 1) {
#pragma unroll
            for (int i = 0; i < 10; ++i) xdma(1, i);
            BARV(20);
        } else BARV(10);
        wread(3);
    }
    if (MODE == M_AQK || MODE == M_AV) wait_ge(a.flags, F_WARM, N_WARM, 2);
    MARK(a, rid, mk0);
    unsigned xa[8];
#pragma unroll
    for (int j = 0; j < 8; ++j) xa[j] = l31 * 1280 + (((2 * j + hi) ^ (l31 & 15)) << 4);
    unsigned d0 = 0, dq[4];
    const rsrc_t rd = mkrsrc(dst, dstbytes);
    if (MODE == M_KQ) d0 = unit * 8192 + ((lane >> 4) & 1) * 4096 + w * 1024 + (hi * 16 + (lane & 15)) * 16;
    if (MODE == M_V) d0 = unit * 8192 + (2 * w + ((lane >> 4) & 1)) * 1024 + (hi * 16 + (lane & 15)) * 16;
    if (MODE == M_AQK) {
#pragma unroll
        for (int i = 0; i < 4; ++i) { const int col = unit * 128 + 32 * w + 8 * i + 4 * hi, sec = col / 640, cc = col % 640, h = cc / 20, d = cc % 20;
            dq[i] = sec * (32 * 2048 * 48) + (h * 2048 + l31) * 48 + d * 2; }
    }
    if (MODE == M_AV) { const int col = unit * 128 + 32 * w + l31 - 1280, h = col / 20, d = col % 20; d0 = h * 64 * 1536 + d * 32 + ((hi ^ (d >> 3)) & 1) * 16; }
    if (MODE == M_AO) d0 = l31 * 2560 + (unit * 128 + 32 * w + 8 * hi) * 2;

    auto epi = [&](const f32x16& acc, const int t) {
        if (MODE == M_KQ || MODE == M_V) {
            const unsigned so = t * (32 * 8192);
            const v4u pa = {pk(acc[0], acc[1]), pk(acc[2], acc[3]), pk(acc[8], acc[9]), pk(acc[10], acc[11])};
            const v4u pb = {pk(acc[4], acc[5]), pk(acc[6], acc[7]), pk(acc[12], acc[13]), pk(acc[14], acc[15])};
            st16<SC1>(pa, rd, d0, so);
            st16<SC1>(pb, rd, d0 + 512, so);
        } else if (MODE == M_AV) {
            const v4u pa = {pk(acc[0], acc[1]), pk(acc[2], acc[3]), pk(acc[4], acc[5]), pk(acc[6], acc[7])};
            const v4u pb = {pk(acc[8], acc[9]), pk(acc[10], acc[11]), pk(acc[12], acc[13]), pk(acc[14], acc[15])};
            st16<SC1>(pa, rd, d0, t * 1536);
            st16<SC1>(pb, rd, d0 + 768, t * 1536);
        } else if (MODE == M_AQK) {
#pragma unroll
            for (int i = 0; i < 4; ++i) {
                const v2u v = {pk(acc[4 * i], acc[4 * i + 1]), pk(acc[4 * i + 2], acc[4 * i + 3])};
                __builtin_amdgcn_raw_buffer_store_b64(v, rd, dq[i], t * (32 * 48), SC1);
            }
        } else {
            const v4u pa = {pk(acc[0], acc[1]), pk(acc[2], acc[3]), pk(acc[4], acc[5]), pk(acc[6], acc[7])};
            const v4u pb = {pk(acc[8], acc[9]), pk(acc[10], acc[11]), pk(acc[12], acc[13]), pk(acc[14], acc[15])};
            st16<A3_AUX>(pa, rd, d0, t * (32 * 2560));
            st16<A3_AUX>(pb, rd, d0 + 32, t * (32 * 2560));
        }
    };
    constexpr int PF = 8;
    f32x16 prev = {};
    for (int it = 0; it < n; ++it) {
        if (it + 1 < n) BARV(10); else BARV(0);
        if (progoff && it >= 2 && lane < 2) stflag(a.flags, progoff + 4 * w + lane * 8192, it - 1);
        const unsigned bo = ((it + 1) % 3) * XBUF;
        const bool more = it + 2 < n;
        const unsigned ldst = lb + (it % 3) * XBUF + w * 1024;
        const char* xsrc = X + (size_t)(t0 + (it + 2) * tstep) * 40960;
        f32x16 acc = {};
        bf16x8 xr[PF];
        auto xread = [&](int kk) { return *(LAS bf16x8*)(lds + bo + xa[kk & 7] + (kk >> 3) * 256); };
#pragma unroll
        for (int j = 0; j < PF; ++j) xr[j] = xread(j);
        __builtin_amdgcn_sched_barrier(0);
#pragma unroll
        for (int kk = 0; kk < 40; ++kk) {
            const bf16x8 xf = xr[kk % PF];
            acc = (MODE == M_V || MODE == M_AV) ? mma32(xf, Wf[kk], acc) : mma32(Wf[kk], xf, acc);
            if (kk + PF < 40) xr[kk % PF] = xread(kk + PF);
            if (kk == 1 && it > 0) epi(prev, t0 + (it - 1) * tstep);
            if (kk >= 6 && kk % 3 == 0 && kk < 36 && more) dma16<COH>(ldst + ((kk - 6) / 3) * 4096, voffX[(kk - 6) / 3], xsrc);
            if (kk == FL_KK && progoff && it >= 1 && it < 4) { if (more) asm volatile("s_waitcnt vmcnt(%0)" :: "n"((FL_KK - 6) / 3 + 1) : "memory"); else WAITV(0); if (lane < 2) stflag(a.flags, progoff + 4 * w + lane * 8192, it); }
            __builtin_amdgcn_sched_barrier(0);
        }
        prev = acc;
        if (it == 5 && mk0 == 1) MARK(a, rid, 9);
    }
    epi(prev, t0 + (n - 1) * tstep);
    BARV(0);
    if (progoff && lane < 2) stflag(a.flags, progoff + 4 * w + lane * 8192, n);
    MARK(a, rid, mk0 + 1);
}

__device__ __forceinline__ unsigned wait_go(char* flags, const unsigned tag) {
    for (unsigned n = 0; n < SPINMAX; ++n) {
        const unsigned v = (unsigned)__builtin_amdgcn_readfirstlane((int)ldflag(flags, F_GO)) - tag - 0x100u;
        if (v < 8u) return v;
        __builtin_amdgcn_s_sleep(4);
    }
    stflag(flags, F_ERR, 3);
    return 0;
}
__device__ __forceinline__ unsigned worker_copy(char* flags, const unsigned tag) { return ((wait_go(flags, tag) & 1u) ^ 1u) << 13; }

__device__ __forceinline__ void t2_role(const Args& a, const unsigned tag, const int cu, const int lane, const int w, const int rid) {
    const int h = 2 * cu + (w >> 1), par = w & 1, u = par * 32 + h;
    const unsigned wc = worker_copy(a.flags, tag);
    asm volatile(T2_ASM :: "s"(a.QX + u * 8192), "s"(a.KX + u * 8192), "s"(a.VX + u * 8192), "s"(a.KM + u * 2048), "s"(a.VM + u * 2048), "s"(a.OX + (par * 64 + h) * 4096),
                 "s"(a.flags), "s"(wc + F_T1 + h * 64), "s"(wc + F_PROG + h * 128 + 12), "s"(wc + F_OP + par * 128 + h * 4), "s"(par), "s"(a.dbg + (1u << 20) + rid * 16), "s"(w) : T2_CLOB);
}

__device__ __forceinline__ void t3_role(const Args& a, const unsigned tag, lchar* lds, const int id, const int lane, const int w, const int rid) {
    const int cg = (id / 40) * 10 + (id % 20) / 2, T = id & 1, par = (id / 20) & 1;
    const unsigned fo = worker_copy(a.flags, tag) + F_OP + par * 128 + 32 * w;
    asm volatile(T3_ASM :: "s"((const char*)a.Wo + (size_t)(1024 * w) * 1280 + cg * 64), "s"(a.OX + ((par * 2 + T) * 32 + 8 * w) * 4096),
                 "s"(a.out + (par * 32 + T * 16) * 2560 + 1280 + cg * 64), "s"(a.flags), "s"(fo), "s"(par), "s"(w), "s"(a.dbg + (1u << 20) + rid * 16) : T3_CLOB);
    __builtin_unreachable();
}

__device__ __forceinline__ void a_attn(const Args& a, const int h, const int w) {
    asm volatile(A2_ASM :: "s"(a.KA + h * (2048 * 48)), "s"(a.VA + h * (64 * 1536)), "s"(a.QA + h * (2048 * 48)), "s"(a.OA + h * 40), "s"(w) : A2_CLOB);
}
__device__ __forceinline__ void arrive(const Args& a, unsigned off, int tid) {
    BARV(0);
    if (tid == 0) __hip_atomic_fetch_add((gu32*)(a.flags + off), 1u, __ATOMIC_RELAXED, __HIP_MEMORY_SCOPE_AGENT);
}
__device__ __forceinline__ Args role_args();
__device__ __forceinline__ void a1_role(lchar* lds, const unsigned tag, const int cg, const int tid, const int lane, const int w, const int rid) {
    const Args a = role_args();
    const char* W = (const char*)a.Awqkv + cg * 256;
    if (cg < 10) wgemm<M_AQK>(a, lds, W, 3840, (const char*)a.Ax, 0, 1, 64, a.QA, 2u * 32u * 2048u * 48u, cg, 0, lane, w, rid, 1, tag);
    else wgemm<M_AV>(a, lds, W, 3840, (const char*)a.Ax, 0, 1, 64, a.VA, 32u * 64u * 1536u, cg, 0, lane, w, rid, 1, tag);
    arrive(a, F_A1, tid);
}
__device__ __forceinline__ void a2_role(lchar* lds, const unsigned tag, const int x, const int k, const int tid, const int lane, const int w, const int rid) {
    {
        const Args a = role_args();
        wait_go(a.flags, tag);
        wait_ge(a.flags, F_A1, 31, 8);
        MARK(a, rid, 3);
        a_attn(a, 8 * x + k, w);
        MARK(a, rid, 4);
        arrive(a, F_A2, tid);
    }
    const Args a = role_args();
    const int id = x * 8 + k;
    if (id < 30) { const int cg = id % 5, part = id / 5; wgemm<M_AO>(a, lds, (const char*)a.Awo + cg * 256, 1280, a.OA, part, 6, part < 4 ? 11 : 10, a.out, 2048u * 2560u, cg, 0, lane, w, rid, 5); }
}

#define NSLOT 2
constexpr int N_FILL = 16;
__device__ __forceinline__ void pref_role(const Args& a, lchar* lds, const int ii, const int lane, const int w) {
    const unsigned lb = (unsigned)(size_t)lds + w * 1024;
    const int h = ii >> 1, half = ii & 1;
    const unsigned vs = (lane >> 4) * 8192 + (lane & 15) * 16;
    const size_t ro = h * 256 + (size_t)(half * 320 + w * 80) * 8192;
    const char* o = (const char*)a.Wo + (size_t)(h * 128 + half * 64 + w * 16) * 1280;
    if (PREF & 8) for (int i = 0; i < 20; ++i) dma16s<(NT_P & 1) != 0>(lb, vs, (const char*)a.Wq + ro + (size_t)i * 32768);
    if (PREF & 1) for (int i = 0; i < 20; ++i) dma16s<(NT_P & 2) != 0>(lb, vs, (const char*)a.Wk + ro + (size_t)i * 32768);
    if (PREF & 2) for (int i = 0; i < 20; ++i) { dma16s<(NT_P & 8) != 0>(lb, lane * 16, o + (size_t)i * 1024); WAITV(56); }
    if (PREF & 4) for (int i = 0; i < 20; ++i) { dma16s<(NT_P & 4) != 0>(lb, vs, (const char*)a.Wv + ro + (size_t)i * 32768); WAITV(56); }
    WAITV(0);
}
__device__ __forceinline__ void fill_role(const Args& a, const unsigned tag, const int ii, const int tid, const int rid) {
    const v4u sent = {SENT, SENT, SENT, SENT}, zero = {0u, 0u, 0u, 0u};
    const rsrc_t rc = mkrsrc(a.comm, 3u * (unsigned)PAGE);
    if (tid < 48) {
        const unsigned p16 = (ii & 7) == 1 && tid == 16 ? (unsigned)(a.w1 & 0xffff) << 4 : 0u;
        st16<SC1>((v4u){p16, p16, p16, p16}, rc, tid * 16, (unsigned)FLAGS_OFF + F_OP + (ii >> 3) * 8192 + (ii & 7) * 768);
    }
    if (ii == 0 && tid >= 64 && tid < 68) stflag(a.flags, tid == 64 ? F_ERR : tid == 65 ? F_A1 : tid == 66 ? F_A2 : F_WARM, 0);
    if (ii >= 12) {
        const int u = ii - 12;
#pragma unroll
        for (int k = 0; k < 4; ++k) {
            const unsigned off = tid * 16 + k * 4096;
            v4u v = sent;
            if (u < 2) {
                const unsigned rel = off & 8191, lin = (u == 0 ? 0 : 8192) + rel;
                if (lin < 10240) v = *(const GAS v4u*)((const GAS char*)a.mem0 + ((lin >> 4) & 7) * 1280 + (lin >> 7) * 16);
                else if (lin == 10240) v.x = 0;
            }
            st16<SC1>(v, rc, ((u & 1) && (k & 1)) ? OOB : off, 2u * (unsigned)PAGE + u * 16384u);
        }
    }
    {
        const rsrc_t rk = mkrsrc(a.KM + 63 * 65536, 65536), rv = mkrsrc(a.VM + 63 * 65536, 65536), ro = mkrsrc(a.OX + (64 - OX_TAGGED) * 262144, OX_TAGGED * 262144);
        st16<SC1>(sent, rk, tid * 16, ii * 4096);
        st16<SC1>(sent, rv, tid * 16, ii * 4096);
#pragma unroll
        for (int k = 0; k < 4 * OX_TAGGED; ++k) st16<SC1>(sent, ro, tid * 16, (ii * 4 * OX_TAGGED + k) * 4096);
    }
    for (int u = ii; u < 160; u += N_FILL) {
        const unsigned base = u < 80 ? u * 16384u : (unsigned)PAGE + (u - 80) * 16384u;
#pragma unroll
        for (int k = 0; k < 4; ++k) st16<SC1>(sent, rc, tid * 16 + k * 4096, base);
    }
    BARV(0);
    if (tid == 0) stflag(a.flags, F_Z + 4 * rid, tag);
}
__device__ __forceinline__ void probe_role(const Args& a, const unsigned tag, const unsigned ep, const int pg, const int lane, const int w) {
    if (w != 0) return;
    char* p = a.comm + (size_t)pg * PAGE + (pg < 2 ? 0x180000 : 20480);
    unsigned best[2] = {~0u, ~0u};
    int r = 0;
    for (; r < NPROBE_MAX; ++r) {
#pragma unroll
        for (int k = 0; k < 2; ++k) {
            const unsigned long long c0 = clk();
            asm volatile("global_store_dword %0, %1, off sc1\n\ts_waitcnt vmcnt(0)" :: "v"(p + k * 8192), "v"(r) : "memory");
            const unsigned long long c1 = clk();
            if (r) best[k] = min(best[k], (unsigned)(c1 - c0));
        }
        if (r >= NPROBE && PRB_D * min(best[0], best[1]) < PRB_N * max(best[0], best[1])) break;
    }
    const unsigned bit = (best[0] < best[1] ? 0u : 1u) ^ ((xcc_id() & 7) >= 4 ? 1u : 0u);
    if (lane == 0) stflag(a.flags, F_PRB + 4 * pg, tag + bit);
    if (pg == 0) {
        for (unsigned n = 0; n < SPINMAX; ++n) {
            bool ok = true;
#pragma unroll
            for (int k = 0; k < 4; ++k) ok = ok && ldflag(a.flags, F_Z + 4 * (lane + 64 * k)) == tag;
            const unsigned pv = ldflag(a.flags, F_PRB + 4 * min(lane, 2)) - tag;
            if (__all(ok && pv < 2u)) {
                const unsigned long long m = __ballot(pv & 1u);
                if (lane == 0) { stflag(a.flags, F_EP, ep + 1u); stflag(a.flags, F_GO, tag + (0x100u | ((unsigned)m & 7u))); }
                break;
            }
            __builtin_amdgcn_s_sleep(2);
        }
    }
}
__device__ __forceinline__ void pull(lchar* lds, const void* src, const int nchunks, const int first, const int step, const int lane, const int w) {
    const unsigned lb = (unsigned)(size_t)lds + w * 1024;
    for (int c = first; c < nchunks; c += step) { dma16<false>(lb, lane * 16, (const char*)src + (size_t)c * 1024); WAITV(48); }
}
template <int PAT>
__device__ __forceinline__ void clear(char* dst, const int nchunks, const int first, const int step, const int lane) {
    const rsrc_t r = mkrsrc(dst, nchunks * 1024);
    for (int c = first; c < nchunks; c += step) {
        v4u z = {0u, 0u, 0u, 0u};
        if (PAT == 1) {
            if (c < 3072) z.z = (c + lane) % 3 == 2 ? 0x3f80u : 0u;
            if (c >= 6144) z = (v4u){0x3f803f80u, 0x3f803f80u, 0x3f803f80u, 0x3f803f80u};
        }
        st16<SC1>(z, r, lane * 16, c * 1024); WAITV(48);
    }
}
constexpr int N_CLR = (2 * 32 * 2048 * 48 + 32 * 64 * 1536) / 1024;
__device__ __forceinline__ void until_us(const unsigned long long t0, const int us) {
    for (unsigned n = 0; n < 100000u && __builtin_amdgcn_s_memrealtime() - t0 < (unsigned long long)(100 * us); ++n) __builtin_amdgcn_s_sleep(4);
}
__device__ __forceinline__ void warm_role(const Args& a, const unsigned long long t0, const unsigned tag, lchar* lds, const int cu, const int tid, const int lane, const int w) {
    if (cu >= 3) pull(lds, a.Bx, 2560, (cu - 3) * 4 + w, 52, lane, w);
    pull(lds, a.Ax, 2560, cu * 4 + w, 64, lane, w);
    if (D_CLR) until_us(t0, D_CLR);
    clear<1>(a.QA, N_CLR, CLR_A + cu * 4 + w, 64, lane);
    wait_go(a.flags, tag);
    arrive(a, F_WARM, tid);
    clear<0>(a.OA, 2560, cu * 4 + w, 64, lane);
    pull(lds, a.Awo, 800, cu * 4 + w, 64, lane, w);
    arrive(a, F_A1, tid);
}
__device__ __forceinline__ void delay_us(const int us) {
    const unsigned long long t0 = __builtin_amdgcn_s_memrealtime();
    for (unsigned n = 0; n < 100000u && __builtin_amdgcn_s_memrealtime() - t0 < (unsigned long long)(100 * us); ++n) __builtin_amdgcn_s_sleep(4);
}

typedef const __attribute__((address_space(4))) Args* kargs_t;
__device__ __forceinline__ Args role_args() {
    kargs_t p = (kargs_t)__builtin_amdgcn_kernarg_segment_ptr();
    asm volatile("" : "+s"(p));
    Args a;
    a.Wq = p->Wq; a.Wk = p->Wk; a.Wv = p->Wv; a.Wo = p->Wo; a.comm = p->comm; a.KX = p->KX; a.VX = p->VX; a.KM = p->KM;
    a.VM = p->VM; a.flags = p->flags; a.dbg = p->dbg; a.w1 = p->w1; a.w2 = p->w2; a.w3 = p->w3; a.w4 = p->w4; a.w5 = p->w5; a.w6 = p->w6;
    a.Ax = p->Ax; a.Awqkv = p->Awqkv; a.Awo = p->Awo; a.Bx = p->Bx; a.mem0 = p->mem0; a.out = p->out; a.QX = p->QX; a.OX = p->OX;
    a.QA = p->QA; a.KA = p->KA; a.VA = p->VA; a.OA = p->OA;
    return a;
}
__global__ void __launch_bounds__(256, 1) mega(Args args) {
    extern __shared__ char lds_[];
    lchar* lds = (lchar*)lds_;
    const int tid = threadIdx.x, lane = tid & 63, w = __builtin_amdgcn_readfirstlane(tid >> 6);
    int role;
    unsigned tag, ep;
    {
        const unsigned x = xcc_id() & 7, j = (blockIdx.x >> 3) & 31;
        const unsigned pref = (x < 4) ? (j < 16 ? x * 16 + j : 64 + x * 16 + (j - 16)) : 128 + (x - 4) * 32 + j;
        LAS int* li = (LAS int*)lds;
        if (w == 1) {
            unsigned sink;
            asm volatile("s_getpc_b64 s[40:41]\nLk_pc:\nv_cmp_gt_i32 vcc, Lk_end-Lk_pc, %1\ns_and_saveexec_b64 s[42:43], vcc\ns_cbranch_execz Lk_skip\n"
                         "global_load_dword %0, %1, s[40:41]\ns_waitcnt vmcnt(0)\nLk_skip:\ns_mov_b64 exec, s[42:43]"
                         : "=&v"(sink) : "v"((j * 64 + lane) * 128) : "s40", "s41", "s42", "s43", "vcc", "scc", "memory");
        }
        if (tid == 0) {
            const unsigned e = ldflag(args.flags, F_EP), t = e * 0x9E3779B1u + 0x7F4A7C15u;
            unsigned r = pref;
            for (int n = 0; n < 256; ++n) { if (__hip_atomic_exchange((gu32*)(args.flags + r * 64), t, __ATOMIC_RELAXED, __HIP_MEMORY_SCOPE_AGENT) != t) break; r = (r + 1) & 255; }
            li[0] = (int)r; li[1] = (int)t; li[2] = (int)e;
            const unsigned jj = (r - 128) & 31;
            if (r < 128 || (jj >= 24 && jj < 28)) stflag(args.flags, F_Z + 4 * r, t);
        }
        __syncthreads();
        role = __builtin_amdgcn_readfirstlane(li[0]);
        tag = (unsigned)__builtin_amdgcn_readfirstlane(li[1]);
        ep = (unsigned)__builtin_amdgcn_readfirstlane(li[2]);
        __syncthreads();
    }
    MARK(args, role, 0);
    if (role < 64) {
        auto kp = __builtin_amdgcn_kernarg_segment_ptr();
        REC_CALL
    } else if (role < 128) {
        pref_role(role_args(), lds, role - 64, lane, w);
        MARK(args, role, 8);
    } else {
        const int x = (role - 128) >> 5, j = (role - 128) & 31;
        const unsigned long long t0 = D_CLR ? __builtin_amdgcn_s_memrealtime() : 0;
        const int k2 = j >= 28 ? j - 24 : (j >= 20 && j < 24 ? j - 20 : -1);
        if (j < 24) {
            {
                const Args a = role_args();
                const int kind = j >> 3, h = 8 * x + (j & 7);
                const unsigned prog = F_T1 + h * 64 + kind * 16;
                if (kind == 2) {
                    if (lane < 2) stflag(a.flags, prog + 4 * w + lane * 8192, 0);
                    BARV(0);
                    if (tid == 0) stflag(a.flags, F_Z + 4 * role, tag);
                    delay_us(D_Q);
                }
                if (kind == 1) wgemm<M_V>(a, lds, (const char*)a.Wv + h * 256, 8192, (const char*)a.Bx, 0, 1, 64, a.VX, 16u << 20, h, prog, lane, w, role, 1, tag);
                else wgemm<M_KQ>(a, lds, (const char*)(kind == 0 ? a.Wk : a.Wq) + h * 256, 8192, (const char*)a.Bx, 0, 1, 64, kind == 0 ? a.KX : a.QX, 16u << 20, h, prog, lane, w, role, 1, tag);
            }
            if (j < 20) {
                t3_role(role_args(), tag, lds, x * 20 + j, lane, w, role);
            }
        } else if (j < 28) {
            const int cu = 4 * x + (j - 24);
            if (cu < 3) { probe_role(role_args(), tag, ep, cu, lane, w); __syncthreads(); }
            warm_role(role_args(), t0, tag, lds, cu, tid, lane, w);
            MARK(args, role, 1);
            t2_role(role_args(), tag, cu, lane, w, role);
        } else {
            const int cg = 4 * x + (j - 28);
            fill_role(role_args(), tag, cg, tid, role);
            MARK(args, role, 8);
            { const Args a = role_args(); if (D_CLR) until_us(t0, D_CLR); clear<1>(a.QA, CLR_A, cg * 4 + w, 64, lane); wait_go(a.flags, tag); arrive(a, F_WARM, tid); }
            delay_us(D_A);
            if (cg < 15) a1_role(lds, tag, cg, tid, lane, w, role);
        }
        if (k2 >= 0) {
            const int t2 = __builtin_amdgcn_mbcnt_hi(-1, __builtin_amdgcn_mbcnt_lo(-1, 0)) + 64 * w;
            a2_role(lds, tag, x, k2, t2, t2 & 63, w, role);
        }
    }
}
}

extern "C" void kernel_launch(void* const* d_in, const int* in_sizes, int n_in, void* d_out, int out_size, void* d_ws, size_t ws_size, hipStream_t st) {
    using namespace mk;
    char* comm = (char*)(((uintptr_t)d_ws + ws_size - (size_t)WS_END * PAGE) & ~(uintptr_t)(PAGE - 1));
    char* p = comm + 3 * PAGE;
    auto take = [&](size_t b) { char* r = p; p += b; return r; };
    Args a;
    a.Wq = (const bf16*)d_in[5]; a.Wk = (const bf16*)d_in[6]; a.Wv = (const bf16*)d_in[7]; a.Wo = (const bf16*)d_in[8];
    a.comm = comm; a.flags = comm + FLAGS_OFF; take(65536);
    a.KX = take(16u << 20); a.VX = take(16u << 20); a.QX = take(16u << 20); a.OX = take(16u << 20);
    a.KM = take(4u << 20); a.VM = take(4u << 20);
    a.QA = take(32u * 2048u * 48u); a.KA = take(32u * 2048u * 48u); a.VA = take(32u * 64u * 1536u); a.OA = take(2048u * 1280u);
    a.dbg = (unsigned*)take(8u << 20);
    a.w1 = P_W1; a.w2 = P_W2; a.w3 = P_W3; a.w4 = P_W4; a.w5 = P_W5; a.w6 = P_W6;
    a.Ax = (const bf16*)d_in[0]; a.Awqkv = (const bf16*)d_in[1]; a.Awo = (const bf16*)d_in[2]; a.Bx = (const bf16*)d_in[3];
    a.mem0 = (const bf16*)d_in[4]; a.out = (char*)d_out;
    (void)hipFuncSetAttribute((const void*)mega, hipFuncAttributeMaxDynamicSharedMemorySize, 160 * 1024);
    hipLaunchKernelGGL(mega, dim3(256), dim3(256), 160 * 1024, st, a);
}
```
